```python
import torch

def _softmax(x, axis=-1):
    return torch.softmax(x, dim=axis)
def _gelu(x):
    return torch.nn.functional.gelu(x, approximate="tanh")
def _silu(x):
    return torch.nn.functional.silu(x)
def _sigmoid(x):
    return torch.sigmoid(x)
def _layernorm(x, axis=-1, eps=1e-5):
    m = x.mean(dim=axis, keepdim=True)
    v = x.var(dim=axis, keepdim=True, unbiased=False)
    return (x - m) / torch.sqrt(v + eps)
def _scatter_add(tgt, idx, src, axis=0):
    return tgt.clone().index_add_(axis, idx, src)
def _segment_sum(src, seg, n):
    out = torch.zeros((n,) + src.shape[1:], dtype=src.dtype)
    return out.index_add_(0, seg, src)
def _pad(x, pw):
    flat = tuple(v for pair in reversed(pw) for v in pair)
    return torch.nn.functional.pad(x, flat)

kernel_name = 'gated_mix__spectral_gfnet__mha_block__prompt'

def setup_inputs():

    _w = (lambda a: (lambda t: t.to(torch.bfloat16) if t.is_floating_point() else t)(torch.as_tensor(a)))
    def _A_setup():
        B, S, D = 1, 2048, 448
        Df = D // 2 + 1
        return {
            "x": _w((lambda *s: torch.randn(*s, dtype=torch.bfloat16) * 0.05)(B, S, D)),
            "w_r": _w((lambda *s: torch.randn(*s, dtype=torch.bfloat16) * 6.4)(S, Df) * 0.02),
            "w_i": _w((lambda *s: torch.randn(*s, dtype=torch.bfloat16) * 6.4)(S, Df) * 0.02),
        }
    _A_in = _A_setup()

    def _B_setup():
        B, S, D, H = 1, 2048, 448, 16
        return {
            "x": _w((lambda *s: torch.randn(*s, dtype=torch.bfloat16) * 0.05)(B, S, D)),
            "wqkv": _w((lambda *s: torch.randn(*s, dtype=torch.bfloat16) * 0.8)(D, 3 * D)),
            "wo": _w((lambda *s: torch.randn(*s, dtype=torch.bfloat16) * 0.05)(D, D)),
        }
    _B_in = _B_setup()

    return {
        "A_x": _A_in["x"],
        "A_w_r": _A_in["w_r"],
        "A_w_i": _A_in["w_i"],
        "B_x": _B_in["x"],
        "B_wqkv": _B_in["wqkv"],
        "B_wo": _B_in["wo"],
        "gate_w": _w((lambda *s: torch.randn(*s, dtype=torch.bfloat16) * 0.05)(448) * 16.0),
    }

def reference(A_x, A_w_r, A_w_i, B_x, B_wqkv, B_wo, gate_w):
    def _A_ref(x, w_r, w_i):
        B, S, D = x.shape
        X = (lambda x, axes=(-2,-1): torch.fft.rfft2(x.float(), dim=axes))(x, axes=(-2, -1))
        W = w_r + 1j * w_i
        X = X * W
        y = (lambda x, s, axes=(-2,-1): torch.fft.irfft2(x, s=s, dim=axes).to(torch.bfloat16))(X, (S, D), axes=(-2, -1))
        return y

    def _B_ref(x, wqkv, wo):
        B, S, D = x.shape
        H = 16
        Dh = D // H
        qkv = (lambda x, *d: x.permute(*d))((x @ wqkv).reshape(B, S, 3, H, Dh), 2, 0, 3, 1, 4)
        q, k, v = qkv[0], qkv[1], qkv[2]
        scores = torch.einsum("bhqd,bhkd->bhqk", q, k) * (Dh ** -0.5)
        p = _softmax(scores, axis=-1)
        o = torch.einsum("bhqk,bhkd->bhqd", p, v)
        return (lambda x, *d: x.permute(*d))(o, 0, 2, 1, 3).reshape(B, S, D) @ wo

    x = A_x
    g = _sigmoid((x * gate_w).sum(-1, keepdims=True))
    a = _A_ref(x, A_w_r, A_w_i)
    b = _B_ref(x, B_wqkv, B_wo)
    return g * a + (1 - g) * b
```

```cpp
#include <hip/hip_runtime.h>
#include <hip/hip_bf16.h>
#include <math.h>
typedef __hip_bfloat16 bf16;
constexpr int S=2048, D=448, DF=225, H=16, DH=28;
__device__ inline float b2f(bf16 v){return __bfloat162float(v);}
__device__ inline float rb(float v){return __bfloat162float(__float2bfloat16(v));}

__global__ void k_qkv(const bf16* x,const bf16* w,float* qkv){
  int n=blockIdx.x*blockDim.x+threadIdx.x; int s=blockIdx.y;
  if(n>=3*D) return;
  float a=0; for(int k=0;k<D;++k) a+=b2f(x[s*D+k])*b2f(w[k*3*D+n]);
  qkv[s*3*D+n]=rb(a);
}
__global__ void k_dftD(const bf16* x,float2* X){
  int k=threadIdx.x; int s=blockIdx.x; if(k>=DF) return;
  float re=0,im=0;
  for(int j=0;j<D;++j){ int t=(j*k)%D; float sn,cs; sincospif(2.0f*t/D,&sn,&cs); float v=b2f(x[s*D+j]); re+=v*cs; im-=v*sn;}
  X[k*S+s]=make_float2(re,im);
}
__global__ void k_dftS(const float2* X,float2* Y,const bf16* wr,const bf16* wi,int inv){
  __shared__ float2 tw[S]; __shared__ float2 xs[S];
  int k=blockIdx.y;
  for(int i=threadIdx.x;i<S;i+=blockDim.x){float sn,cs; sincospif(2.0f*i/S,&sn,&cs); tw[i]=make_float2(cs,inv?sn:-sn); xs[i]=X[k*S+i];}
  __syncthreads();
  int f=blockIdx.x*blockDim.x+threadIdx.x;
  float re=0,im=0;
  for(int s=0;s<S;++s){ float2 t=tw[(s*f)&(S-1)]; float2 v=xs[s]; re+=v.x*t.x-v.y*t.y; im+=v.x*t.y+v.y*t.x;}
  if(!inv){ float a=b2f(wr[f*DF+k]),b=b2f(wi[f*DF+k]); float r2=re*a-im*b,i2=re*b+im*a; re=r2;im=i2;}
  Y[k*S+f]=make_float2(re,im);
}
__global__ void k_idftD(const float2* Y,float* a){
  int j=threadIdx.x; int s=blockIdx.x; if(j>=D) return;
  float acc=0;
  for(int k=0;k<DF;++k){ int t=(j*k)%D; float sn,cs; sincospif(2.0f*t/D,&sn,&cs); float2 v=Y[k*S+s];
    float c=(k==0||k==DF-1)?1.f:2.f; float im=(k==0||k==DF-1)?0.f:v.y; acc+=c*(v.x*cs-im*sn);}
  a[s*D+j]=rb(acc/(float)(S*D));
}
__global__ void k_attn(const float* qkv,float* o){
  __shared__ float p[S]; __shared__ float red[256];
  int h=blockIdx.y,q=blockIdx.x,t=threadIdx.x;
  float qv[DH]; for(int d=0;d<DH;++d) qv[d]=qkv[q*3*D+h*DH+d];
  float m=-1e30f;
  for(int s=t;s<S;s+=256){ float a=0; for(int d=0;d<DH;++d) a+=qv[d]*qkv[s*3*D+D+h*DH+d]; a=rb(rb(a)*rb(0.1889822365046136f)); p[s]=a; m=fmaxf(m,a);}
  red[t]=m; __syncthreads(); for(int o2=128;o2>0;o2>>=1){ if(t<o2) red[t]=fmaxf(red[t],red[t+o2]); __syncthreads();} m=red[0]; __syncthreads();
  float l=0; for(int s=t;s<S;s+=256){ float e=expf(p[s]-m); p[s]=e; l+=e;}
  red[t]=l; __syncthreads(); for(int o2=128;o2>0;o2>>=1){ if(t<o2) red[t]+=red[t+o2]; __syncthreads();} l=red[0]; __syncthreads();
  if(t<DH){ float a=0; for(int s=0;s<S;++s) a+=rb(p[s]/l)*qkv[s*3*D+2*D+h*DH+t]; o[q*D+h*DH+t]=rb(a);}
}
__global__ void k_final(const bf16* x,const bf16* gw,const float* o,const bf16* wo,const float* a,bf16* out){
  int n=threadIdx.x; int s=blockIdx.x; if(n>=D) return;
  float z=0; for(int k=0;k<D;++k) z+=rb(b2f(x[s*D+k])*b2f(gw[k])); z=rb(z);
  float g=rb(1.f/(1.f+expf(-z)));
  float b=0; for(int k=0;k<D;++k) b+=o[s*D+k]*b2f(wo[k*D+n]); b=rb(b);
  float r=rb(g*a[s*D+n])+rb(rb(1.f-g)*b);
  out[s*D+n]=__float2bfloat16(r);
}
extern "C" void kernel_launch(void* const* d_in, const int* in_sizes, int n_in,
                              void* d_out, int out_size, void* d_ws, size_t ws_size,
                              hipStream_t stream) {
  const bf16* x=(const bf16*)d_in[0]; const bf16* wr=(const bf16*)d_in[1]; const bf16* wi=(const bf16*)d_in[2];
  const bf16* wqkv=(const bf16*)d_in[4]; const bf16* wo=(const bf16*)d_in[5]; const bf16* gw=(const bf16*)d_in[6];
  char* ws=(char*)d_ws;
  float* qkv=(float*)ws; ws+=S*3*D*4;
  float2* X=(float2*)ws; ws+=DF*S*8;
  float2* Y=(float2*)ws; ws+=DF*S*8;
  float* a=(float*)ws; ws+=S*D*4;
  float* o=(float*)ws; ws+=S*D*4;
  k_qkv<<<dim3(6,S),256,0,stream>>>(x,wqkv,qkv);
  k_dftD<<<S,256,0,stream>>>(x,X);
  k_dftS<<<dim3(8,DF),256,0,stream>>>(X,Y,wr,wi,0);
  k_dftS<<<dim3(8,DF),256,0,stream>>>(Y,X,wr,wi,1);
  k_idftD<<<S,448,0,stream>>>(X,a);
  k_attn<<<dim3(S,H),256,0,stream>>>(qkv,o);
  k_final<<<S,448,0,stream>>>(x,gw,o,wo,a,(bf16*)d_out);
}
```

```cpp
#include <hip/hip_runtime.h>
#include <stdint.h>

typedef short bf16x8 __attribute__((ext_vector_type(8)));
typedef short s16x4 __attribute__((ext_vector_type(4)));
typedef float f32x16 __attribute__((ext_vector_type(16)));
typedef float f32x4 __attribute__((ext_vector_type(4)));
typedef float f32x2 __attribute__((ext_vector_type(2)));
typedef unsigned u32x4 __attribute__((ext_vector_type(4)));
typedef __bf16 bf16x2_t __attribute__((ext_vector_type(2)));
#define DI __device__ __forceinline__
#define LDS_AS __attribute__((address_space(3)))

namespace tab {
constexpr double PI = 3.14159265358979323846264338327950288;
constexpr double tsin(double x) { double x2 = x * x, t = x, s = x; for (int i = 1; i <= 10; ++i) { t *= -x2 / ((2 * i) * (2 * i + 1)); s += t; } return s; }
constexpr double tcos(double x) { double x2 = x * x, t = 1, s = 1; for (int i = 1; i <= 10; ++i) { t *= -x2 / ((2 * i - 1) * (2 * i)); s += t; } return s; }
constexpr double cosr(int m, int N) {
  m %= N; if (m < 0) m += N; if (2 * m > N) m = N - m;
  bool neg = false; if (4 * m > N) { m = N / 2 - m; neg = true; }
  double r = (8 * m > N) ? tsin(2 * PI * (N / 4 - m) / N) : tcos(2 * PI * m / N); return neg ? -r : r; }
constexpr double sinr(int m, int N) { return cosr(m - N / 4, N); }
constexpr uint32_t f2bf(double d) { float f = (float)d; uint32_t u = __builtin_bit_cast(uint32_t, f); u += 0x7fffu + ((u >> 16) & 1u); return u >> 16; }
constexpr int crow(int r, int hh) { return (r & 3) + 8 * (r >> 2) + 4 * hh; }
constexpr int krow(int s, int hh, int j) { return 16 * s + 8 * (j >> 2) + 4 * hh + (j & 3); }
constexpr int sigma(int r) { return 16 * ((r >> 2) & 1) + 4 * (r >> 3) + (r & 3); }
struct Base448 { uint16_t c[448], ns[448]; };
constexpr Base448 mk448() { Base448 b{}; for (int i = 0; i < 448; ++i) { b.c[i] = f2bf(cosr(i, 448)); b.ns[i] = f2bf(-sinr(i, 448)); } return b; }
constexpr Base448 B448 = mk448();
constexpr uint32_t Fval(int j, int c) { return c == 1 ? B448.c[(j * 224) % 448] : ((c & 1) ? B448.ns[(j * (c >> 1)) % 448] : B448.c[(j * (c >> 1)) % 448]); }
struct Tile { uint32_t v[28 * 64 * 4]; };
constexpr Tile mkF(int ct) { Tile t{}; for (int ks = 0; ks < 28; ++ks) for (int l = 0; l < 64; ++l) { int c = ct * 32 + (l & 31), j0 = ks * 16 + 8 * (l >> 5);
  for (int e = 0; e < 4; ++e) t.v[(ks * 64 + l) * 4 + e] = Fval(j0 + 2 * e, c) | (Fval(j0 + 2 * e + 1, c) << 16); } return t; }
constexpr Tile mkG(int jt) { Tile t{}; for (int ks = 0; ks < 28; ++ks) for (int l = 0; l < 64; ++l) { int j = jt * 32 + sigma(l & 31), c0 = ks * 16 + 8 * (l >> 5);
  for (int e = 0; e < 4; ++e) t.v[(ks * 64 + l) * 4 + e] = Fval(j, c0 + 2 * e) | (Fval(j, c0 + 2 * e + 1) << 16); } return t; }
template <int T> constexpr Tile FT = mkF(T);
template <int T> constexpr Tile GT = mkG(T);
struct Tab14 { Tile t[14]; };
struct Fft { uint32_t t1[12][256]; float tw[8][256]; uint32_t t64[24][256]; uint32_t t4[12][256]; };
struct Base64 { uint16_t v[3][64]; };
constexpr Base64 mk64() { Base64 b{}; for (int i = 0; i < 64; ++i) { b.v[0][i] = f2bf(cosr(i, 64)); b.v[1][i] = f2bf(sinr(i, 64)); b.v[2][i] = f2bf(-sinr(i, 64)); } return b; }
constexpr Base64 B64 = mk64();
constexpr uint32_t cs64(int m, int which) { return B64.v[which][m & 63]; }
constexpr Fft mkFft() { Fft f{};
  for (int l = 0; l < 64; ++l) { const int r = l & 31, hh = l >> 5;
    for (int e = 0; e < 4; ++e) {
      for (int t = 0; t < 2; ++t) for (int w = 0; w < 3; ++w) for (int s = 0; s < 2; ++s) {
        const int k0 = krow(s, hh, 2 * e), k1 = krow(s, hh, 2 * e + 1), n1 = sigma(r);
        f.t1[(t * 3 + w) * 2 + s][l * 4 + e] = cs64((2 * k0 + t) * r, w) | (cs64((2 * k1 + t) * r, w) << 16);
        f.t4[(t * 3 + w) * 2 + s][l * 4 + e] = cs64((2 * n1 + t) * k0, w) | (cs64((2 * n1 + t) * k1, w) << 16); }
      for (int t = 0; t < 2; ++t) for (int w = 0; w < 3; ++w) for (int ks = 0; ks < 4; ++ks) {
        const int a = 32 * t + r, b0 = 32 * (ks >> 1) + krow(ks & 1, hh, 2 * e), b1 = 32 * (ks >> 1) + krow(ks & 1, hh, 2 * e + 1);
        f.t64[(t * 3 + w) * 4 + ks][l * 4 + e] = cs64(a * b0, w) | (cs64(a * b1, w) << 16); }
      for (int g = 0; g < 4; ++g) { const int m = crow(4 * g + e, hh) * r; f.tw[g][l * 4 + e] = (float)cosr(m, 2048); f.tw[4 + g][l * 4 + e] = (float)sinr(m, 2048); }
    } }
  return f; }
constexpr Fft FFT = mkFft();
}
#define TAB14(X) {{tab::X<0>, tab::X<1>, tab::X<2>, tab::X<3>, tab::X<4>, tab::X<5>, tab::X<6>, tab::X<7>, tab::X<8>, tab::X<9>, tab::X<10>, tab::X<11>, tab::X<12>, tab::X<13>}}
__device__ __attribute__((aligned(256))) const tab::Tab14 g_ftab = TAB14(FT);
__device__ __attribute__((aligned(256))) const tab::Tab14 g_gtab = TAB14(GT);
__device__ __attribute__((aligned(256))) const tab::Fft g_fft = tab::FFT;

constexpr size_t WS_Q = 0;
constexpr size_t WS_KV = WS_Q + (2u << 20);
constexpr size_t WS_X = WS_KV + (4u << 20);
constexpr size_t WS_WT = WS_X + (2u << 20);
constexpr size_t WS_G = WS_WT + (2u << 20);
constexpr size_t WS_O = WS_G + (1u << 20);
constexpr size_t WS_Y = WS_O + (2u << 20);

#define MFMA(a, b, c) __builtin_amdgcn_mfma_f32_32x32x16_bf16((a), (b), (c), 0, 0, 0)
#define SBAR() __builtin_amdgcn_sched_barrier(0)
#define PIN(x) asm volatile("" : "+v"(x))
DI unsigned pk(float lo, float hi) { f32x2 v = {lo, hi}; return __builtin_bit_cast(unsigned, __builtin_convertvector(v, bf16x2_t)); }
DI bf16x8 asbf(u32x4 v) { return __builtin_bit_cast(bf16x8, v); }
template <int S> DI u32x4 pack8(const f32x16& a) { return (u32x4){pk(a[8 * S], a[8 * S + 1]), pk(a[8 * S + 2], a[8 * S + 3]), pk(a[8 * S + 4], a[8 * S + 5]), pk(a[8 * S + 6], a[8 * S + 7])}; }
DI float rbf(float v) { return __uint_as_float(pk(v, v) << 16); }
DI void glds(unsigned lds, unsigned voff, const void* sbase) {
  asm volatile("s_mov_b32 m0, %0\n\ts_nop 0\n\tglobal_load_lds_dwordx4 %1, %2" ::"s"(lds), "v"(voff), "s"(sbase) : "memory"); }
typedef short v4i16_t __attribute__((ext_vector_type(4)));
DI bf16x8 lds_b128(unsigned a) { return *(const LDS_AS bf16x8*)(size_t)a; }
DI bf16x8 lds_tr2(unsigned a0, unsigned a1) {
  v4i16_t lo = __builtin_amdgcn_ds_read_tr16_b64_v4i16((LDS_AS v4i16_t*)(size_t)a0), hi = __builtin_amdgcn_ds_read_tr16_b64_v4i16((LDS_AS v4i16_t*)(size_t)a1);
  return __builtin_shufflevector(lo, hi, 0, 1, 2, 3, 4, 5, 6, 7); }
DI float halfmax(float m) { auto r = __builtin_amdgcn_permlane32_swap(__float_as_uint(m), __float_as_uint(m), false, false); return fmaxf(__uint_as_float(r[0]), __uint_as_float(r[1])); }
DI float max16(const f32x16& s) { float m = fmaxf(s[0], s[1]);
#pragma unroll
  for (int i = 2; i < 16; i += 2) m = fmaxf(fmaxf(m, s[i]), s[i + 1]);
  return m; }

constexpr int K1_SLOT = 32768;
template <int WC> DI void k1_chunk(unsigned xa, unsigned wa, unsigned fa, f32x16 (&acc)[2][2]) {
#pragma unroll
  for (int ks = 0; ks < 4; ++ks) {
    const bf16x8 x0 = lds_b128(xa ^ (ks * 32)), x1 = lds_b128((xa ^ (ks * 32)) + 4096);
    const bf16x8 w0 = lds_tr2(wa + ks * 1024, wa + ks * 1024 + 256);
    if (WC == 0) {
      const bf16x8 w1 = lds_tr2(wa + 4096 + ks * 1024, wa + 4096 + ks * 1024 + 256);
      acc[0][0] = MFMA(w0, x0, acc[0][0]); acc[1][0] = MFMA(w0, x1, acc[1][0]);
      acc[0][1] = MFMA(w1, x0, acc[0][1]); acc[1][1] = MFMA(w1, x1, acc[1][1]);
    } else {
      const bf16x8 w1 = lds_b128(fa + ks * 1024);
      acc[0][0] = MFMA(x0, w0, acc[0][0]); acc[1][0] = MFMA(x1, w0, acc[1][0]);
      acc[0][1] = MFMA(x0, w1, acc[0][1]); acc[1][1] = MFMA(x1, w1, acc[1][1]);
    }
  }
}

__global__ __launch_bounds__(256, 1) void k1_kernel(const char* __restrict__ x, const char* __restrict__ wqkv, const char* __restrict__ gw,
                                                     const char* __restrict__ wr, const char* __restrict__ wi, char* __restrict__ ws) {
  __shared__ __attribute__((aligned(1024))) char lds[5 * K1_SLOT];
  const int tid = threadIdx.x, lane = tid & 63, r = lane & 31, hh = lane >> 5;
  const int w = __builtin_amdgcn_readfirstlane(tid >> 6);
  const int b = blockIdx.x, xcd = b & 7, bi = b >> 3;
  const int t = 4 * (xcd >> 1) + (bi & 3), h = 8 * (xcd & 1) + (bi >> 2);
  const unsigned l0 = (unsigned)(size_t)lds;

  const int wo_ = bi & 3, wfq = 2 * xcd + ((bi >> 2) & 1), wkq = bi >> 3;
  const int wk = wkq * 57 + lane; const bool wvalid = lane < 57 && wk < 225;
  unsigned wv[16];
  {
    const unsigned base = (unsigned)((8 * wo_ + 2 * w + 128 * wfq) * 225 + (wvalid ? wk : 0)) * 2u;
#pragma unroll
    for (int e = 0; e < 4; ++e) {
      const unsigned o = base + e * (32 * 450);
      asm volatile("global_load_ushort %0, %4, %5\n\tglobal_load_ushort %1, %4, %6\n\tglobal_load_ushort %2, %4, %5 offset:450\n\tglobal_load_ushort %3, %4, %6 offset:450"
                   : "=&v"(wv[e]), "=&v"(wv[4 + e]), "=&v"(wv[8 + e]), "=&v"(wv[12 + e]) : "v"(o), "s"(wr), "s"(wi) : "memory");
    }
  }
  const unsigned xo_e = (unsigned)(lane >> 3) * (64u * 896u) + (unsigned)(((lane & 7) ^ (lane >> 4)) * 16);
  const unsigned xo_o = xo_e ^ 64u;
  const char* xb = x + (size_t)(4 * t + w) * 896;
  unsigned co; const char* cb; unsigned sj, sc;
  if (w < 3) { co = (unsigned)(lane >> 2) * 2688u + (unsigned)(lane & 3) * 16u; cb = wqkv + (size_t)(w * 448 + 28 * h - 4 * (h & 1)) * 2; sj = 16 * 2688; sc = 64 * 2688; }
  else if (h < 14) { co = lane * 16u; cb = (const char*)&g_ftab + (size_t)h * 28672; sj = 1024; sc = 4096; }
  else { co = hh * 16u; cb = gw; sj = 32; sc = 128; }
  auto issue = [&](int c, int slot) {
    const unsigned lb = l0 + slot * K1_SLOT + w * 4096;
#pragma unroll
    for (int j = 0; j < 4; ++j) {
      glds(lb + j * 1024, (j & 1) ? xo_o : xo_e, xb + c * 128 + j * (8 * 64 * 896));
      glds(lb + 16384 + j * 1024, co, cb + c * sc + j * sj);
    }
  };
#pragma unroll
  for (int c = 0; c < 5; ++c) issue(c, c);
  asm volatile("s_waitcnt vmcnt(40)" : "+v"(wv[0]), "+v"(wv[1]), "+v"(wv[2]), "+v"(wv[3]), "+v"(wv[4]), "+v"(wv[5]), "+v"(wv[6]), "+v"(wv[7]),
               "+v"(wv[8]), "+v"(wv[9]), "+v"(wv[10]), "+v"(wv[11]), "+v"(wv[12]), "+v"(wv[13]), "+v"(wv[14]), "+v"(wv[15]));
  if (wvalid) {
    u32x4* dst = (u32x4*)(ws + WS_WT + (size_t)wk * 8192 + (size_t)(wfq >> 1) * 1024 + (size_t)(32 * (wfq & 1) + 8 * wo_ + 2 * w) * 16);
    dst[0] = (u32x4){wv[0] | (wv[4] << 16), wv[1] | (wv[5] << 16), wv[2] | (wv[6] << 16), wv[3] | (wv[7] << 16)};
    dst[1] = (u32x4){wv[8] | (wv[12] << 16), wv[9] | (wv[13] << 16), wv[10] | (wv[14] << 16), wv[11] | (wv[15] << 16)};
  }
  const int wr_ = w & 1, wc = w >> 1;
  f32x16 acc[2][2] = {};
  const unsigned xa = l0 + wr_ * 8192 + r * 128 + ((hh ^ ((r >> 1) & 7)) * 16);
  const unsigned wa = l0 + 16384 + wc * 8192 + (8 * hh + ((lane & 15) >> 2)) * 64 + ((lane >> 4) & 1) * 32 + (lane & 3) * 8;
  const unsigned fa = l0 + 16384 + 12288 + lane * 16;
#define K1_STEP(C, SLOT, N, ISS)                                                       \
  asm volatile("s_waitcnt vmcnt(" #N ") lgkmcnt(0)\n\ts_barrier" ::: "memory");        \
  if (ISS >= 0) issue(ISS, ISS - 5);                                                   \
  if (wc == 0) k1_chunk<0>(xa + SLOT * K1_SLOT, wa + SLOT * K1_SLOT, fa, acc);         \
  else k1_chunk<1>(xa + SLOT * K1_SLOT, wa + SLOT * K1_SLOT, fa + SLOT * K1_SLOT, acc);
  K1_STEP(0, 0, 32, -1)
  K1_STEP(1, 1, 24, 5)
  K1_STEP(2, 2, 24, 6)
  K1_STEP(3, 3, 24, -1)
  K1_STEP(4, 4, 16, -1)
  K1_STEP(5, 0, 8, -1)
  K1_STEP(6, 1, 0, -1)
  const bool odd = h & 1;
  if (wc == 0) {
    const bool padl = odd ? (hh == 0) : (hh == 1);
#pragma unroll
    for (int rt = 0; rt < 2; ++rt) {
      const int tile = 4 * t + 2 * wr_ + rt;
      f32x16 q = acc[rt][0] * (0.1889822365046136f * 1.4426950408889634f);
      u32x4 q0 = pack8<0>(q), q1 = pack8<1>(q), k0 = pack8<0>(acc[rt][1]), k1 = pack8<1>(acc[rt][1]);
      if (odd) { if (padl) { q0[0] = 0; q0[1] = 0; k0[0] = 0x3F80u; k0[1] = 0; } }
      else     { if (padl) { q1[2] = 0; q1[3] = 0; k1[2] = 0x3F80u; k1[3] = 0; } }
      f32x16 s = {};
      s = MFMA(asbf(k0), asbf(q0), s); s = MFMA(asbf(k1), asbf(q1), s);
      const float m = halfmax(max16(s));
      const unsigned nm = pk(-m, 0.f);
      if (odd) { if (padl) q0[0] = nm; } else { if (padl) q1[2] = nm; }
      u32x4* qd = (u32x4*)(ws + WS_Q + (size_t)(h * 64 + tile) * 2048) + lane;
      qd[0] = q0; qd[64] = q1;
      u32x4* kd = (u32x4*)(ws + WS_KV + (size_t)(h * 64 + tile) * 4096) + lane;
      kd[0] = k0; kd[64] = k1;
    }
  } else {
    const int d = (r - (odd ? 4 : 0)) & 31;
    const unsigned padv = d == 28 ? 0x3F803F80u : 0u;
#pragma unroll
    for (int rt = 0; rt < 2; ++rt) {
      const int tile = 4 * t + 2 * wr_ + rt;
      u32x4 v0 = pack8<0>(acc[rt][0]), v1 = pack8<1>(acc[rt][0]);
      if (d >= 28) { v0 = (u32x4){padv, padv, padv, padv}; v1 = v0; }
      u32x4* vd = (u32x4*)(ws + WS_KV + (size_t)(h * 64 + tile) * 4096 + 2048) + (d + 32 * hh);
      vd[0] = v0; vd[64] = v1;
      if (h < 14) {
        u32x4* xd = (u32x4*)(ws + WS_X + (size_t)(32 * h + r) * 4096 + (size_t)(tile >> 5) * 2048) + ((tile & 31) + 32 * hh);
        xd[0] = pack8<0>(acc[rt][1]); xd[64] = pack8<1>(acc[rt][1]);
      } else if (h == 14) {
        if (r < 4) {
          f32x4 z;
#pragma unroll
          for (int e = 0; e < 4; ++e) { float v = r == 0 ? acc[rt][1][e] : r == 1 ? acc[rt][1][4 + e] : r == 2 ? acc[rt][1][8 + e] : acc[rt][1][12 + e];
            z[e] = rbf(1.f / (1.f + __expf(-rbf(v)))); }
          *(f32x4*)(ws + WS_G + (size_t)(tile * 32 + 8 * r + 4 * hh) * 4) = z;
        }
      }
    }
  }
}

#define EX(x) x = __builtin_amdgcn_exp2f(x)
DI f32x16 lds_f16(unsigned a) { f32x16 v;
#pragma unroll
  for (int g = 0; g < 4; ++g) { const f32x4 c = *(const LDS_AS f32x4*)(size_t)(a + g * 1024);
#pragma unroll
    for (int e = 0; e < 4; ++e) v[4 * g + e] = c[e]; }
  return v; }
DI void fft_wave(int u, int lane, char* __restrict__ ws, unsigned l0) {
  const bool special = (u == 0) | (u == 224);
  const int cr = u == 224 ? 1 : 2 * u;
  const unsigned vo = lane * 16u;
  { const char* xs = ws + WS_X + (size_t)cr * 4096; const char* ts = (const char*)&g_fft; const char* wsrc = ws + WS_WT + (size_t)u * 8192;
    for (int i = 0; i < 8; ++i) glds(l0 + i * 1024, vo, xs + i * 1024);
    for (int i = 0; i < 44; ++i) glds(l0 + (8 + i) * 1024, vo, ts + i * 1024);
    for (int i = 0; i < 8; ++i) glds(l0 + (52 + i) * 1024, vo, wsrc + i * 1024);
    for (int i = 0; i < 12; ++i) glds(l0 + (60 + i) * 1024, vo, ts + (44 + i) * 1024); }
  const unsigned L = l0 + vo;
#define FR(i) lds_b128(L + (i) * 1024)
  asm volatile("s_waitcnt vmcnt(44)" ::: "memory");
  u32x4 ar[4], ai[4];
  const unsigned zmask = special ? 0u : ~0u;
#pragma unroll
  for (int rt = 0; rt < 2; ++rt) {
    f32x16 re = {}, im = {};
#pragma unroll
    for (int s = 0; s < 2; ++s) {
      const bf16x8 zr = FR(rt * 2 + s), zi = asbf(__builtin_bit_cast(u32x4, FR(4 + rt * 2 + s)) & zmask);
      const bf16x8 C = FR(8 + rt * 6 + s), S = FR(8 + rt * 6 + 2 + s), N = FR(8 + rt * 6 + 4 + s);
      re = MFMA(zr, C, re); im = MFMA(zi, C, im);
      re = MFMA(zi, S, re); im = MFMA(zr, N, im);
    }
    const f32x16 twc = lds_f16(L + 20 * 1024), tws = lds_f16(L + 24 * 1024);
    const f32x16 r2 = re * twc + im * tws, i2 = im * twc - re * tws;
    ar[rt * 2] = pack8<0>(r2); ar[rt * 2 + 1] = pack8<1>(r2); ai[rt * 2] = pack8<0>(i2); ai[rt * 2 + 1] = pack8<1>(i2);
    SBAR();
  }
  asm volatile("s_waitcnt vmcnt(12)" ::: "memory");
  u32x4 ur[4], ui[4];
#pragma unroll
  for (int ft = 0; ft < 2; ++ft) {
    f32x16 re = {}, im = {};
#pragma unroll
    for (int ks = 0; ks < 4; ++ks) {
      const bf16x8 C = FR(28 + ft * 12 + ks), S = FR(28 + ft * 12 + 4 + ks), N = FR(28 + ft * 12 + 8 + ks);
      re = MFMA(C, asbf(ar[ks]), re); im = MFMA(C, asbf(ai[ks]), im);
      re = MFMA(S, asbf(ai[ks]), re); im = MFMA(N, asbf(ar[ks]), im);
    }
    f32x16 wr_, wi_;
#pragma unroll
    for (int g = 0; g < 4; ++g) { const u32x4 v = __builtin_bit_cast(u32x4, FR(52 + ft * 4 + g));
#pragma unroll
      for (int e = 0; e < 4; ++e) { wr_[4 * g + e] = __uint_as_float(v[e] << 16); wi_[4 * g + e] = __uint_as_float(v[e] & 0xffff0000u); } }
    const f32x16 r2 = re * wr_ - im * wi_, i2 = re * wi_ + im * wr_;
    ur[ft * 2] = pack8<0>(r2); ur[ft * 2 + 1] = pack8<1>(r2); ui[ft * 2] = pack8<0>(i2); ui[ft * 2 + 1] = pack8<1>(i2);
    SBAR();
  }
  asm volatile("s_waitcnt vmcnt(0)" ::: "memory");
  const float osc = special ? 0.5f : 1.f;
#pragma unroll
  for (int ct = 0; ct < 2; ++ct) {
    f32x16 re = {}, im = {};
#pragma unroll
    for (int ks = 0; ks < 4; ++ks) {
      const bf16x8 C = FR(28 + ct * 12 + ks), S = FR(28 + ct * 12 + 4 + ks), N = FR(28 + ct * 12 + 8 + ks);
      re = MFMA(asbf(ur[ks]), C, re); im = MFMA(asbf(ui[ks]), C, im);
      re = MFMA(asbf(ui[ks]), N, re); im = MFMA(asbf(ur[ks]), S, im);
    }
    const f32x16 twc = lds_f16(L + 20 * 1024), tws = lds_f16(L + 24 * 1024);
    const f32x16 r2 = re * twc - im * tws, i2 = im * twc + re * tws;
    const u32x4 br[2] = {pack8<0>(r2), pack8<1>(r2)}, bi[2] = {pack8<0>(i2), pack8<1>(i2)};
    f32x16 yr = {}, yi = {};
#pragma unroll
    for (int s = 0; s < 2; ++s) {
      const bf16x8 C = FR(60 + ct * 6 + s), S = FR(60 + ct * 6 + 2 + s), N = FR(60 + ct * 6 + 4 + s);
      yr = MFMA(C, asbf(br[s]), yr); yi = MFMA(C, asbf(bi[s]), yi);
      yr = MFMA(N, asbf(bi[s]), yr); yi = MFMA(S, asbf(br[s]), yi);
    }
    yr *= osc;
    u32x4* yd = (u32x4*)(ws + WS_Y + (size_t)cr * 4096 + (size_t)ct * 2048 + (size_t)(lane & 31) * 64 + (size_t)(lane >> 5) * 32);
    yd[0] = pack8<0>(yr); yd[1] = pack8<1>(yr);
    if (!special) { yd[256] = pack8<0>(yi); yd[257] = pack8<1>(yi); }
    SBAR();
  }
}

DI void attn_slow(int h, int tile, int lane, char* __restrict__ ws) {
  const int hh = lane >> 5; const bool odd = h & 1;
  const u32x4* qp = (const u32x4*)(ws + WS_Q + (size_t)(h * 64 + tile) * 2048) + lane;
  u32x4 q0 = qp[0], q1 = qp[64];
  if (odd) { if (hh == 0) q0[0] = 0; } else { if (hh == 1) q1[2] = 0; }
  const u32x4* kv = (const u32x4*)(ws + WS_KV + (size_t)h * 262144) + lane;
  float m = -INFINITY;
  for (int i = 0; i < 64; ++i) {
    f32x16 s = {}; s = MFMA(asbf(kv[i * 256]), asbf(q0), s); s = MFMA(asbf(kv[i * 256 + 64]), asbf(q1), s);
    m = fmaxf(m, max16(s));
  }
  m = halfmax(m);
  f32x16 o = {};
  for (int i = 0; i < 64; ++i) {
    f32x16 s = {}; s = MFMA(asbf(kv[i * 256]), asbf(q0), s); s = MFMA(asbf(kv[i * 256 + 64]), asbf(q1), s);
#pragma unroll
    for (int j = 0; j < 16; ++j) s[j] = __builtin_amdgcn_exp2f(s[j] - m);
    o = MFMA(asbf(kv[i * 256 + 128]), asbf(pack8<0>(s)), o); o = MFMA(asbf(kv[i * 256 + 192]), asbf(pack8<1>(s)), o);
  }
  auto rr = __builtin_amdgcn_permlane32_swap(__float_as_uint(o[12]), __float_as_uint(o[12]), false, false);
  const float rl = 1.f / __uint_as_float(rr[1]);
  o *= rl;
  u32x4 o0 = pack8<0>(o), o1 = pack8<1>(o);
  if (hh == 1) { o1[2] = 0; o1[3] = 0; }
  u32x4* od = (u32x4*)(ws + WS_O + (size_t)(tile * 16 + h) * 2048) + lane;
  od[0] = o0; od[64] = o1;
}

__global__ __launch_bounds__(576, 1) void k2_kernel(char* __restrict__ ws) {
  __shared__ __attribute__((aligned(1024))) char lds[54272 + 73728];
  const int tid = threadIdx.x, lane = tid & 63, hh = lane >> 5;
  const int w = __builtin_amdgcn_readfirstlane(tid >> 6);
  const int b = blockIdx.x;
  if (w == 8) { if (b < 225) fft_wave(b, lane, ws, (unsigned)(size_t)lds + 54272); return; }
  const int h = 2 * (b & 7) + ((b >> 3) & 1), t = b >> 4;
  const int qg = w >> 2, ks = w & 3;
  const int t0 = qg == 0 ? (ks == 0 ? 0 : ks == 1 ? 12 : ks == 2 ? 30 : 46) : (ks == 0 ? 0 : ks == 1 ? 14 : ks == 2 ? 30 : 48);
  const int t1 = qg == 0 ? (ks == 0 ? 12 : ks == 1 ? 30 : ks == 2 ? 46 : 64) : (ks == 0 ? 14 : ks == 1 ? 30 : ks == 2 ? 48 : 64);
  const u32x4* qp = (const u32x4*)(ws + WS_Q + (size_t)(h * 64 + 4 * t + 2 * qg) * 2048) + lane;
  const u32x4* kv = (const u32x4*)(ws + WS_KV + (size_t)h * 262144) + lane;
  u32x4 kA0 = kv[t0 * 256], kA1 = kv[t0 * 256 + 64];
  const u32x4 qa0 = qp[0], qa1 = qp[64], qb0 = qp[128], qb1 = qp[192];
  u32x4 vA0 = kv[t0 * 256 + 128], vA1 = kv[t0 * 256 + 192];
  u32x4 kB0 = kv[t0 * 256 + 256], kB1 = kv[t0 * 256 + 320], vB0 = kv[t0 * 256 + 384], vB1 = kv[t0 * 256 + 448];
  f32x16 oa = {}, ob = {}, sa, sb;
  const f32x16 zero = {};
  sa = MFMA(asbf(kA0), asbf(qa0), zero); sa = MFMA(asbf(kA1), asbf(qa1), sa);
  u32x4 p0, p1;
#define HALF(K0, K1, V0, V1, KN0, KN1, NXT)                                                                                   \
  do {                                                                                                                        \
    SBAR();                                                                                                                   \
    sb = MFMA(asbf(K0), asbf(qb0), zero); SBAR();                                                                             \
    EX(sa[0]); EX(sa[1]); EX(sa[2]); EX(sa[3]); PIN(sa); SBAR();                                                              \
    sb = MFMA(asbf(K1), asbf(qb1), sb); SBAR();                                                                               \
    K0 = kv[(NXT) * 256]; K1 = kv[(NXT) * 256 + 64];                                                                          \
    EX(sa[4]); EX(sa[5]); EX(sa[6]); EX(sa[7]); p0 = pack8<0>(sa); EX(sa[8]); EX(sa[9]); PIN(p0); PIN(sa); SBAR();            \
    oa = MFMA(asbf(V0), asbf(p0), oa); SBAR();                                                                                \
    EX(sa[10]); EX(sa[11]); EX(sa[12]); EX(sa[13]); EX(sa[14]); EX(sa[15]); p1 = pack8<1>(sa); PIN(p1); SBAR();               \
    oa = MFMA(asbf(V1), asbf(p1), oa); SBAR();                                                                                \
    sa = MFMA(asbf(KN0), asbf(qa0), zero); SBAR();                                                                            \
    EX(sb[0]); EX(sb[1]); EX(sb[2]); EX(sb[3]); PIN(sb); SBAR();                                                              \
    sa = MFMA(asbf(KN1), asbf(qa1), sa); SBAR();                                                                              \
    EX(sb[4]); EX(sb[5]); EX(sb[6]); EX(sb[7]); p0 = pack8<0>(sb); EX(sb[8]); EX(sb[9]); PIN(p0); PIN(sb); SBAR();            \
    ob = MFMA(asbf(V0), asbf(p0), ob); SBAR();                                                                                \
    EX(sb[10]); EX(sb[11]); EX(sb[12]); EX(sb[13]); EX(sb[14]); EX(sb[15]); p1 = pack8<1>(sb); PIN(p1); SBAR();               \
    ob = MFMA(asbf(V1), asbf(p1), ob); SBAR();                                                                                \
    V0 = kv[(NXT) * 256 + 128]; V1 = kv[(NXT) * 256 + 192];                                                                   \
  } while (0)
  for (int i = t0; i < t1; i += 2) {
    HALF(kA0, kA1, vA0, vA1, kB0, kB1, (i + 2) & 63);
    HALF(kB0, kB1, vB0, vB1, kA0, kA1, (i + 3) & 63);
  }
  bool bad;
  {
    const bool odd = h & 1;
    const unsigned ma = odd ? qa0[0] : qa1[2], mb = odd ? qb0[0] : qb1[2];
    const bool padl = odd ? (hh == 0) : (hh == 1);
    bad = padl && ((ma & 0x7fffu) > 0x4580u || (mb & 0x7fffu) > 0x4580u);
    bad |= (hh == 1) && !(oa[12] <= 1.0e12f && ob[12] <= 1.0e12f);
    float mx = 0.f;
#pragma unroll
    for (int i = 0; i < 16; ++i) mx = fmaxf(mx, fmaxf(fabsf(oa[i]), fabsf(ob[i])));
    float sum = 0.f;
#pragma unroll
    for (int i = 0; i < 16; ++i) sum += oa[i] * 0.f + ob[i] * 0.f;
    bad |= !(mx <= 1.0e37f) || !(sum == 0.f);
  }
  f32x4* cw = (f32x4*)(lds + qg * 24576) + lane;
#pragma unroll
  for (int dst = 0; dst < 4; ++dst) {
    if (dst != ks) {
      const int slot = dst * 3 + (ks > dst ? ks - 1 : ks);
      const f32x16& o = dst < 2 ? oa : ob; const int o8 = (dst & 1) * 8;
      cw[slot * 128] = (f32x4){o[o8], o[o8 + 1], o[o8 + 2], o[o8 + 3]};
      cw[slot * 128 + 64] = (f32x4){o[o8 + 4], o[o8 + 5], o[o8 + 6], o[o8 + 7]};
    }
  }
  float* lw = (float*)(lds + 49152);
  if (hh == 1) { lw[w * 64 + (lane & 31)] = oa[12]; lw[w * 64 + 32 + (lane & 31)] = ob[12]; }
  unsigned* fw = (unsigned*)(lds + 49152 + 4096);
  { const bool anyb = __any(bad); if (lane == 0) fw[w] = anyb; }
  __syncthreads();
  const u32x4 f0 = *(const u32x4*)fw, f1 = *(const u32x4*)(fw + 4);
  if (__builtin_expect((f0[0] | f0[1] | f0[2] | f0[3] | f1[0] | f1[1] | f1[2] | f1[3]) != 0, 0)) {
    if (w < 4) attn_slow(h, 4 * t + w, lane, ws);
    return;
  }
  f32x4 m0, m1;
  if (ks == 0) { m0 = (f32x4){oa[0], oa[1], oa[2], oa[3]}; m1 = (f32x4){oa[4], oa[5], oa[6], oa[7]}; }
  else if (ks == 1) { m0 = (f32x4){oa[8], oa[9], oa[10], oa[11]}; m1 = (f32x4){oa[12], oa[13], oa[14], oa[15]}; }
  else if (ks == 2) { m0 = (f32x4){ob[0], ob[1], ob[2], ob[3]}; m1 = (f32x4){ob[4], ob[5], ob[6], ob[7]}; }
  else { m0 = (f32x4){ob[8], ob[9], ob[10], ob[11]}; m1 = (f32x4){ob[12], ob[13], ob[14], ob[15]}; }
#pragma unroll
  for (int j = 0; j < 3; ++j) { m0 += cw[(ks * 3 + j) * 128]; m1 += cw[(ks * 3 + j) * 128 + 64]; }
  const float* lr = lw + qg * 256 + (ks >> 1) * 32 + (lane & 31);
  const float rl = 1.f / (lr[0] + lr[64] + lr[128] + lr[192]);
  m0 *= rl; m1 *= rl;
  u32x4 ov = {pk(m0[0], m0[1]), pk(m0[2], m0[3]), pk(m1[0], m1[1]), pk(m1[2], m1[3])};
  if ((ks & 1) && hh == 1) { ov[2] = 0; ov[3] = 0; }
  *((u32x4*)(ws + WS_O + (size_t)((4 * t + 2 * qg + (ks >> 1)) * 16 + h) * 2048 + (size_t)(ks & 1) * 1024) + lane) = ov;
}

#define GLD(dst, voff, sb, imm) asm volatile("global_load_dwordx4 %0, %1, %2 offset:" #imm : "=v"(dst) : "v"(voff), "s"(sb) : "memory")
__global__ __launch_bounds__(256, 1) void k3_kernel(const char* __restrict__ wo, const char* __restrict__ ws, char* __restrict__ out) {
  __shared__ __attribute__((aligned(1024))) char lds[2 * 57344 + 16384];
  const int tid = threadIdx.x, lane = tid & 63, r = lane & 31, hh = lane >> 5;
  const int w = __builtin_amdgcn_readfirstlane(tid >> 6);
  const int b = blockIdx.x, rb = 4 * (b & 7) + (b >> 3) / 7, cb = (b >> 3) % 7;
  const unsigned l0 = (unsigned)(size_t)lds;
  const bool isb = w >= 2; const int dt = w & 1;
  const unsigned rs = isb ? 896u : 4096u;
  const unsigned so = (unsigned)(lane >> 3) * rs + (unsigned)(((lane & 7) ^ (4 * ((lane >> 4) & 1))) * 16);
  const char* sb = (isb ? wo + cb * 128 : ws + WS_Y + rb * 128) + (size_t)dt * 56 * rs;
  const unsigned lb = l0 + (isb ? 57344 : 0) + dt * 7168;
  const char* db = isb ? ws + WS_O + (size_t)(2 * rb + dt) * 32768 : (const char*)&g_gtab + (size_t)(2 * cb + dt) * 28672;
  const unsigned dst_ = isb ? 8192u : 7168u, vo = lane * 16u;
  u32x4 f[4][8];
#pragma unroll
  for (int st = 0; st < 4; ++st) {
#pragma unroll
    for (int j = 0; j < 7; ++j) glds(lb + st * 14336 + j * 1024, so, sb + (size_t)(st * 112 + j * 8) * rs);
    const char* d0 = db + st * dst_; const char* d1 = d0 + 4096;
    GLD(f[st][0], vo, d0, 0); GLD(f[st][1], vo, d0, 1024); GLD(f[st][2], vo, d0, 2048); GLD(f[st][3], vo, d0, 3072);
    GLD(f[st][4], vo, d1, 0); GLD(f[st][5], vo, d1, 1024); GLD(f[st][6], vo, d1, 2048);
    if (isb) GLD(f[st][7], vo, d1, 3072); else GLD(f[st][7], vo, d1, 2048);
  }
  const int frt = dt, fct = isb ? 1 - dt : dt;
  float g; asm volatile("global_load_dword %0, %1, %2" : "=v"(g) : "v"(r * 4u), "s"(ws + WS_G + (size_t)(rb * 64 + frt * 32) * 4) : "memory");
  f32x16 acc0 = {}, acc1 = {};
  const int q = (lane & 15) >> 2, p = lane & 3, G = (lane >> 4) & 1;
  const unsigned ya = l0 + (8 * hh + q) * 128 + ((q >> 1) * 64) + G * 32 + p * 8;
  const unsigned wa = l0 + 57344 + (4 * hh + q) * 128 + ((q >> 1) * 64) + (4 * (p & 1) + 2 * G + (p >> 1)) * 8;
  const unsigned wa2 = wa - hh * 512;
#define K3_STAGE(ST, N)                                                                                                                  \
  asm volatile("s_waitcnt vmcnt(" #N ")\n\ts_barrier" : "+v"(f[ST][0]), "+v"(f[ST][1]), "+v"(f[ST][2]), "+v"(f[ST][3]), "+v"(f[ST][4]), \
               "+v"(f[ST][5]), "+v"(f[ST][6]), "+v"(f[ST][7])::"memory");                                                                \
  if (!isb) {                                                                                                                            \
    _Pragma("unroll") for (int j = 0; j < 7; ++j) {                                                                                      \
      const unsigned a = ya + (ST * 7 + j) * 2048;                                                                                       \
      acc0 = MFMA(asbf(f[ST][j]), lds_tr2(a, a + 512), acc0);                                                                            \
      acc1 = MFMA(asbf(f[ST][j]), lds_tr2(a ^ 64, (a ^ 64) + 512), acc1);                                                                \
    }                                                                                                                                    \
  } else {                                                                                                                               \
    _Pragma("unroll") for (int j = 0; j < 8; ++j) {                                                                                      \
      const unsigned o = (ST * 4 + (j >> 1)) * 3584 + (j & 1) * 2048;                                                                    \
      const unsigned a = wa + o, a2 = ((j & 1) ? wa2 : wa) + o + 1024;                                                                   \
      acc0 = MFMA(lds_tr2(a, a2), asbf(f[ST][j]), acc0);                                                                                 \
      acc1 = MFMA(lds_tr2(a ^ 64, a2 ^ 64), asbf(f[ST][j]), acc1);                                                                       \
    }                                                                                                                                    \
  }
  K3_STAGE(0, 46)
  K3_STAGE(1, 31)
  K3_STAGE(2, 16)
  K3_STAGE(3, 0)
  asm volatile("" : "+v"(g));
  const bool keep1 = isb ? (dt == 0) : (dt == 1);
  const f32x16 mine = keep1 ? acc1 : acc0, send = keep1 ? acc0 : acc1;
  f32x4* ex = (f32x4*)(lds + 114688) + lane;
  const int to = isb ? dt : 3 - dt;
#pragma unroll
  for (int i = 0; i < 4; ++i) ex[to * 256 + i * 64] = (f32x4){send[4 * i], send[4 * i + 1], send[4 * i + 2], send[4 * i + 3]};
  __syncthreads();
  f32x16 other;
#pragma unroll
  for (int i = 0; i < 4; ++i) { const f32x4 v = ex[w * 256 + i * 64]; other[4 * i] = v[0]; other[4 * i + 1] = v[1]; other[4 * i + 2] = v[2]; other[4 * i + 3] = v[3]; }
  const f32x16 av = isb ? other : mine, bv = isb ? mine : other;
  const float g1 = rbf(1.f - g);
  f32x16 res;
#pragma unroll
  for (int i = 0; i < 16; ++i) res[i] = rbf(g * rbf(av[i] * (2.0f / (2048.f * 448.f)))) + rbf(g1 * rbf(bv[i]));
  const int srow = 64 * r + 2 * rb + frt;
  u32x4* od = (u32x4*)(out + (size_t)srow * 896 + (size_t)(cb * 64 + fct * 32 + 16 * hh) * 2);
  od[0] = pack8<0>(res); od[1] = pack8<1>(res);
}

extern "C" void kernel_launch(void* const* d_in, const int* in_sizes, int n_in, void* d_out, int out_size, void* d_ws, size_t ws_size, hipStream_t stream) {
  char* ws = (char*)d_ws;
  k1_kernel<<<256, 256, 0, stream>>>((const char*)d_in[0], (const char*)d_in[4], (const char*)d_in[6], (const char*)d_in[1], (const char*)d_in[2], ws);
  k2_kernel<<<256, 576, 0, stream>>>(ws);
  k3_kernel<<<224, 256, 0, stream>>>((const char*)d_in[5], ws, (char*)d_out);
}
```

```cpp
#include <hip/hip_runtime.h>
#include <stdint.h>

typedef short bf16x8 __attribute__((ext_vector_type(8)));
typedef short s16x4 __attribute__((ext_vector_type(4)));
typedef float f32x16 __attribute__((ext_vector_type(16)));
typedef float f32x4 __attribute__((ext_vector_type(4)));
typedef float f32x2 __attribute__((ext_vector_type(2)));
typedef unsigned u32x4 __attribute__((ext_vector_type(4)));
typedef __bf16 bf16x2_t __attribute__((ext_vector_type(2)));
#define K1_MAP 1
#define DI __device__ __forceinline__
#define LDS_AS __attribute__((address_space(3)))

namespace tab {
constexpr double PI = 3.14159265358979323846264338327950288;
constexpr double tsin(double x) { double x2 = x * x, t = x, s = x; for (int i = 1; i <= 10; ++i) { t *= -x2 / ((2 * i) * (2 * i + 1)); s += t; } return s; }
constexpr double tcos(double x) { double x2 = x * x, t = 1, s = 1; for (int i = 1; i <= 10; ++i) { t *= -x2 / ((2 * i - 1) * (2 * i)); s += t; } return s; }
constexpr double cosr(int m, int N) {
  m %= N; if (m < 0) m += N; if (2 * m > N) m = N - m;
  bool neg = false; if (4 * m > N) { m = N / 2 - m; neg = true; }
  double r = (8 * m > N) ? tsin(2 * PI * (N / 4 - m) / N) : tcos(2 * PI * m / N); return neg ? -r : r; }
constexpr double sinr(int m, int N) { return cosr(m - N / 4, N); }
constexpr uint32_t f2bf(double d) { float f = (float)d; uint32_t u = __builtin_bit_cast(uint32_t, f); u += 0x7fffu + ((u >> 16) & 1u); return u >> 16; }
constexpr int crow(int r, int hh) { return (r & 3) + 8 * (r >> 2) + 4 * hh; }
constexpr int krow(int s, int hh, int j) { return 16 * s + 8 * (j >> 2) + 4 * hh + (j & 3); }
constexpr int sigma(int r) { return 16 * ((r >> 2) & 1) + 4 * (r >> 3) + (r & 3); }
struct Base448 { uint16_t c[448], ns[448]; };
constexpr Base448 mk448() { Base448 b{}; for (int i = 0; i < 448; ++i) { b.c[i] = f2bf(cosr(i, 448)); b.ns[i] = f2bf(-sinr(i, 448)); } return b; }
constexpr Base448 B448 = mk448();
constexpr uint32_t Fval(int j, int c) { return c == 1 ? B448.c[(j * 224) % 448] : ((c & 1) ? B448.ns[(j * (c >> 1)) % 448] : B448.c[(j * (c >> 1)) % 448]); }
struct Tile { uint32_t v[28 * 64 * 4]; };
constexpr Tile mkF(int jt) { Tile t{}; for (int j = 0; j < 32; ++j) for (int c = 0; c < 224; ++c) t.v[j * 224 + c] = Fval(jt * 32 + j, 2 * c) | (Fval(jt * 32 + j, 2 * c + 1) << 16); return t; }
constexpr Tile mkG(int jt) { Tile t{}; for (int ks = 0; ks < 28; ++ks) for (int l = 0; l < 64; ++l) { int j = jt * 32 + sigma(l & 31), c0 = ks * 16 + 8 * (l >> 5);
  for (int e = 0; e < 4; ++e) t.v[(ks * 64 + l) * 4 + e] = Fval(j, c0 + 2 * e) | (Fval(j, c0 + 2 * e + 1) << 16); } return t; }
template <int T> constexpr Tile FT = mkF(T);
template <int T> constexpr Tile GT = mkG(T);
struct Tab14 { Tile t[14]; };
struct Fft { uint32_t t1[12][256]; float tw[8][256]; uint32_t t64[24][256]; uint32_t t4[12][256]; };
struct Base64 { uint16_t v[3][64]; };
constexpr Base64 mk64() { Base64 b{}; for (int i = 0; i < 64; ++i) { b.v[0][i] = f2bf(cosr(i, 64)); b.v[1][i] = f2bf(sinr(i, 64)); b.v[2][i] = f2bf(-sinr(i, 64)); } return b; }
constexpr Base64 B64 = mk64();
constexpr uint32_t cs64(int m, int which) { return B64.v[which][m & 63]; }
constexpr Fft mkFft() { Fft f{};
  for (int l = 0; l < 64; ++l) { const int r = l & 31, hh = l >> 5;
    for (int e = 0; e < 4; ++e) {
      for (int t = 0; t < 2; ++t) for (int w = 0; w < 3; ++w) for (int s = 0; s < 2; ++s) {
        const int k0 = krow(s, hh, 2 * e), k1 = krow(s, hh, 2 * e + 1), n1 = sigma(r);
        f.t1[(t * 3 + w) * 2 + s][l * 4 + e] = cs64((2 * k0 + t) * r, w) | (cs64((2 * k1 + t) * r, w) << 16);
        f.t4[(t * 3 + w) * 2 + s][l * 4 + e] = cs64((2 * n1 + t) * k0, w) | (cs64((2 * n1 + t) * k1, w) << 16); }
      for (int t = 0; t < 2; ++t) for (int w = 0; w < 3; ++w) for (int ks = 0; ks < 4; ++ks) {
        const int a = 32 * t + r, b0 = 32 * (ks >> 1) + krow(ks & 1, hh, 2 * e), b1 = 32 * (ks >> 1) + krow(ks & 1, hh, 2 * e + 1);
        f.t64[(t * 3 + w) * 4 + ks][l * 4 + e] = cs64(a * b0, w) | (cs64(a * b1, w) << 16); }
      for (int g = 0; g < 4; ++g) { const int m = crow(4 * g + e, hh) * r; f.tw[g][l * 4 + e] = (float)cosr(m, 2048); f.tw[4 + g][l * 4 + e] = (float)sinr(m, 2048); }
    } }
  return f; }
constexpr Fft FFT = mkFft();
}
#define TAB14(X) {{tab::X<0>, tab::X<1>, tab::X<2>, tab::X<3>, tab::X<4>, tab::X<5>, tab::X<6>, tab::X<7>, tab::X<8>, tab::X<9>, tab::X<10>, tab::X<11>, tab::X<12>, tab::X<13>}}
__device__ __attribute__((aligned(256))) const tab::Tab14 g_ftab = TAB14(FT);
__device__ __attribute__((aligned(256))) const tab::Tab14 g_gtab = TAB14(GT);
__device__ __attribute__((aligned(256))) const tab::Fft g_fft = tab::FFT;

constexpr size_t WS_Q = 0;
constexpr size_t WS_KV = WS_Q + (2u << 20);
constexpr size_t WS_X = WS_KV + (4u << 20);
constexpr size_t WS_WT = WS_X + (2u << 20);
constexpr size_t WS_G = WS_WT + (2u << 20);
constexpr size_t WS_O = WS_G + (1u << 20);
constexpr size_t WS_Y = WS_O + (2u << 20);

#define STAMP_DECL
#define STAMP(i)
#define STAMP_OUT(K, W)
#define CSTAMP(i)
#define MFMA(a, b, c) __builtin_amdgcn_mfma_f32_32x32x16_bf16((a), (b), (c), 0, 0, 0)
#define SBAR() __builtin_amdgcn_sched_barrier(0)
#define PIN(x) asm volatile("" : "+v"(x))
DI unsigned pk(float lo, float hi) { f32x2 v = {lo, hi}; return __builtin_bit_cast(unsigned, __builtin_convertvector(v, bf16x2_t)); }
DI bf16x8 asbf(u32x4 v) { return __builtin_bit_cast(bf16x8, v); }
template <int S> DI u32x4 pack8(const f32x16& a) { return (u32x4){pk(a[8 * S], a[8 * S + 1]), pk(a[8 * S + 2], a[8 * S + 3]), pk(a[8 * S + 4], a[8 * S + 5]), pk(a[8 * S + 6], a[8 * S + 7])}; }
DI float rbf(float v) { return __uint_as_float(pk(v, v) << 16); }
DI void glds(unsigned lds, unsigned voff, const void* sbase) {
  asm volatile("s_mov_b32 m0, %0\n\ts_nop 0\n\tglobal_load_lds_dwordx4 %1, %2" ::"s"(lds), "v"(voff), "s"(sbase) : "memory"); }
DI void glds4(unsigned lds, unsigned voff, const void* sbase) {
  asm volatile("s_mov_b32 m0, %0\n\ts_nop 0\n\tglobal_load_lds_dwordx4 %1, %2\n\tglobal_load_lds_dwordx4 %1, %2 offset:1024\n\t"
               "global_load_lds_dwordx4 %1, %2 offset:2048\n\tglobal_load_lds_dwordx4 %1, %2 offset:3072" ::"s"(lds), "v"(voff), "s"(sbase) : "memory"); }
DI void touch(unsigned lds, unsigned voff, const void* sbase) {
  asm volatile("s_mov_b32 m0, %0\n\ts_nop 0\n\tglobal_load_lds_dword %1, %2" ::"s"(lds), "v"(voff), "s"(sbase) : "memory"); }
#define AUX1 16
#define AUX2 16
#define AUX3 0
typedef unsigned v4u_t __attribute__((ext_vector_type(4)));
template <int AUX> DI void st16(void* p, u32x4 v) {
  if (AUX == 0) *(u32x4*)p = v;
  else if (AUX == 2) asm volatile("global_store_dwordx4 %0, %1, off nt\n\ts_nop 1" ::"v"(p), "v"(v) : "memory");
  else if (AUX == 16) asm volatile("global_store_dwordx4 %0, %1, off sc1\n\ts_nop 1" ::"v"(p), "v"(v) : "memory");
  else asm volatile("global_store_dwordx4 %0, %1, off sc0 sc1\n\ts_nop 1" ::"v"(p), "v"(v) : "memory");
}
#define ENDPGM() __builtin_amdgcn_endpgm()
typedef short v4i16_t __attribute__((ext_vector_type(4)));
DI bf16x8 lds_b128(unsigned a) { return *(const LDS_AS bf16x8*)(size_t)a; }
DI bf16x8 lds_tr2(unsigned a0, unsigned a1) {
  v4i16_t lo = __builtin_amdgcn_ds_read_tr16_b64_v4i16((LDS_AS v4i16_t*)(size_t)a0), hi = __builtin_amdgcn_ds_read_tr16_b64_v4i16((LDS_AS v4i16_t*)(size_t)a1);
  return __builtin_shufflevector(lo, hi, 0, 1, 2, 3, 4, 5, 6, 7); }
DI float halfmax(float m) { auto r = __builtin_amdgcn_permlane32_swap(__float_as_uint(m), __float_as_uint(m), false, false); return fmaxf(__uint_as_float(r[0]), __uint_as_float(r[1])); }
DI float max16(const f32x16& s) { float m = fmaxf(s[0], s[1]);
#pragma unroll
  for (int i = 2; i < 16; i += 2) m = fmaxf(fmaxf(m, s[i]), s[i + 1]);
  return m; }

constexpr int K1_SLOT = 32768;
struct K1Frag { bf16x8 x0, x1, w0, w1; };
struct K1Addr { unsigned x[4], w0, w1; };
DI void k1_read(K1Frag& f, const K1Addr& A, unsigned o, int ks) {
  f.x0 = lds_b128(A.x[ks] + o); f.w0 = lds_tr2(A.w0 + o + ks * 2048, A.w0 + o + ks * 2048 + 512);
  f.x1 = lds_b128(A.x[ks] + o + 4096); f.w1 = lds_tr2(A.w1 + o + ks * 2048, A.w1 + o + ks * 2048 + 512);
}
template <int TR> DI void k1_mma(const K1Frag& f, f32x16 (&acc)[2][2]) {
  if (TR) { acc[0][0] = MFMA(f.w0, f.x0, acc[0][0]); acc[1][0] = MFMA(f.w0, f.x1, acc[1][0]); acc[0][1] = MFMA(f.w1, f.x0, acc[0][1]); acc[1][1] = MFMA(f.w1, f.x1, acc[1][1]); }
  else    { acc[0][0] = MFMA(f.x0, f.w0, acc[0][0]); acc[1][0] = MFMA(f.x1, f.w0, acc[1][0]); acc[0][1] = MFMA(f.x0, f.w1, acc[0][1]); acc[1][1] = MFMA(f.x1, f.w1, acc[1][1]); }
}
#define K1_STAMP_ARG
#define K1_STAMP_PASS
template <int TR> DI void k1_compute(unsigned xa, unsigned wa, f32x16 (&acc)[2][2] K1_STAMP_ARG) {
  K1Addr A;
#pragma unroll
  for (int ks = 0; ks < 4; ++ks) A.x[ks] = xa ^ (ks * 32);
  A.w0 = wa; A.w1 = wa ^ 64;
  K1Frag fa, fb = {};
#define K1_RD(F, KS, M0, M1, M2, M3, G) do {                                                                                     \
    F.x0 = lds_b128(A.x[KS]); SBAR(); M0(G); SBAR();                                                                              \
    F.w0 = lds_tr2(A.w0 + KS * 2048, A.w0 + KS * 2048 + 512); if (KS == 3) { A.w0 += d; PIN(A.w0); } SBAR(); M1(G); SBAR();       \
    F.x1 = lds_b128(A.x[KS] + 4096); A.x[KS] += d; PIN(A.x[KS]); SBAR(); M2(G); SBAR();                                           \
    F.w1 = lds_tr2(A.w1 + KS * 2048, A.w1 + KS * 2048 + 512); if (KS == 3) { A.w1 += d; PIN(A.w1); } SBAR(); M3(G); SBAR(); } while (0)
#define K1_M0(G) acc[0][0] = TR ? MFMA(G.w0, G.x0, acc[0][0]) : MFMA(G.x0, G.w0, acc[0][0])
#define K1_M1(G) acc[1][0] = TR ? MFMA(G.w0, G.x1, acc[1][0]) : MFMA(G.x1, G.w0, acc[1][0])
#define K1_M2(G) acc[0][1] = TR ? MFMA(G.w1, G.x0, acc[0][1]) : MFMA(G.x0, G.w1, acc[0][1])
#define K1_M3(G) acc[1][1] = TR ? MFMA(G.w1, G.x1, acc[1][1]) : MFMA(G.x1, G.w1, acc[1][1])
  __builtin_amdgcn_s_waitcnt(0xC07F);
#pragma unroll 1
  for (int i = 0; i < 7; ++i) {
    asm volatile("s_barrier" ::: "memory");
    const unsigned d = i == 4 ? (unsigned)(-4 * K1_SLOT) : (unsigned)K1_SLOT;
    K1_RD(fa, 0, K1_M0, K1_M1, K1_M2, K1_M3, fb);
    K1_RD(fb, 1, K1_M0, K1_M1, K1_M2, K1_M3, fa);
    K1_RD(fa, 2, K1_M0, K1_M1, K1_M2, K1_M3, fb);
    K1_RD(fb, 3, K1_M0, K1_M1, K1_M2, K1_M3, fa);
  }
  k1_mma<TR>(fb, acc);
  PIN(acc[0][0]); PIN(acc[1][1]); STAMP(2); CSTAMP(9);
}

__global__ __launch_bounds__(512, 2) void k1_kernel(const char* __restrict__ x, const char* __restrict__ wqkv, const char* __restrict__ gw,
                                                     const char* __restrict__ wr, const char* __restrict__ wi, char* __restrict__ ws) {
  __shared__ __attribute__((aligned(1024))) char lds[5 * K1_SLOT];
  const int tid = threadIdx.x, lane = tid & 63, r = lane & 31, hh = lane >> 5;
  const int w8 = __builtin_amdgcn_readfirstlane(tid >> 6), w = w8 & 3;
  const int b = blockIdx.x, xcd = b & 7, bi = b >> 3;
  const unsigned l0 = (unsigned)(size_t)lds;
  STAMP_DECL; STAMP(0);
  if (bi >= 28) {
    const int id = xcd * 4 + (bi - 28), fq = id >> 1, f1h = id & 1;
    { const char* src = (w8 < 4 ? wr : wi) + (size_t)(128 * fq + 32 * w + 16 * f1h) * 450;
      const unsigned lb = l0 + w8 * 8192;
      glds4(lb, lane * 16u, src);
      glds(lb + 4096, lane * 16u, src + 4096); glds(lb + 5120, lane * 16u, src + 5120); glds(lb + 6144, lane * 16u, src + 6144);
      glds(lb + 7168, (lane < 2 ? lane : 0) * 16u, src + 7168); }
    { const int p = 64 * id + 8 * w8 + (lane >> 3);
      const u32x4* xr = (const u32x4*)(x + (size_t)(64 * (p & 31) + (p >> 5)) * 896) + (lane & 7);
      const u32x4* gr = (const u32x4*)gw + (lane & 7);
      u32x4 gx[7], gv[7];
#pragma unroll
      for (int i = 0; i < 7; ++i) { gx[i] = xr[8 * i]; gv[i] = gr[8 * i]; }
      if (bi == 28 && w8 < 7) touch(l0 + 65536 + w8 * 256, (w8 * 64 + lane) * 128u, &g_fft);
      float z = 0.f;
#pragma unroll
      for (int i = 0; i < 7; ++i)
#pragma unroll
        for (int e = 0; e < 4; ++e) { z = fmaf(__uint_as_float(gx[i][e] << 16), __uint_as_float(gv[i][e] << 16), z); z = fmaf(__uint_as_float(gx[i][e] & 0xffff0000u), __uint_as_float(gv[i][e] & 0xffff0000u), z); }
      z += __shfl_xor(z, 1); z += __shfl_xor(z, 2); z += __shfl_xor(z, 4);
      if ((lane & 7) == 0) *(float*)(ws + WS_G + (size_t)p * 4) = rbf(1.f / (1.f + __expf(-rbf(z)))); }
    asm volatile("s_waitcnt vmcnt(0)\n\ts_barrier" ::: "memory");
    { const int f1l = tid & 15, k0 = tid >> 4;
      const LDS_AS unsigned short* src = (const LDS_AS unsigned short*)(size_t)(l0 + f1l * 450 + k0 * 2);
      char* dst = ws + WS_WT + (size_t)k0 * 8192 + (size_t)(fq >> 1) * 1024 + (size_t)(32 * (fq & 1) + 16 * f1h + f1l) * 16;
      u32x4 v[8];
#pragma unroll
      for (int it = 0; it < 8; ++it)
#pragma unroll
        for (int e = 0; e < 4; ++e) v[it][e] = (unsigned)src[e * 4096 + it * 32] | ((unsigned)src[16384 + e * 4096 + it * 32] << 16);
#pragma unroll
      for (int it = 0; it < 8; ++it) if (it < 7 || k0 == 0) st16<AUX1>(dst + (size_t)it * 32 * 8192, v[it]);
    }
    STAMP(9); STAMP(11); STAMP_OUT(0, w8);
    ENDPGM();
  }
  const int t = 4 * (xcd >> 1) + (bi & 3), g = 7 * (xcd & 1) + (bi >> 2);
  if (w8 < 4) {
    const int rot = (g + 2 * t) % 7;
    const unsigned xo_e = (unsigned)(lane >> 3) * (64u * 896u) + (unsigned)(((lane & 7) ^ (lane >> 4)) * 16);
    const unsigned xo_o = xo_e ^ 64u;
    const char* xb = x + (size_t)(4 * t + w) * 896;
    const int c0 = 128 * g + 64 * (w >> 1);
    const unsigned rs = c0 < 1344 ? 2688u : 896u;
    const char* cb = (c0 < 1344 ? wqkv + c0 * 2 : (const char*)&g_ftab + (c0 - 1344) * 2) + (size_t)(32 * (w & 1)) * rs;
    const unsigned co = (unsigned)(lane >> 3) * rs + (unsigned)(((lane & 7) ^ (4 * ((lane >> 4) & 1))) * 16);
    auto issue = [&](int i) __attribute__((always_inline)) {
      int c = i + rot; c = c >= 7 ? c - 7 : c;
      const unsigned lb = l0 + (i % 5) * K1_SLOT + w * 4096;
      const char* xc = xb + c * 128; const char* cc = cb + (size_t)(c * 64) * rs;
#pragma unroll
      for (int j = 0; j < 4; ++j) {
        glds(lb + j * 1024, (j & 1) ? xo_o : xo_e, xc + j * (8 * 64 * 896));
        glds(lb + 16384 + j * 1024, co, cc + j * 8 * rs);
      }
    };
    issue(0); issue(1);
    STAMP(1);
    asm volatile("s_waitcnt vmcnt(8)\n\ts_barrier" ::: "memory");
    STAMP(2);
    issue(2); issue(3);
    asm volatile("s_waitcnt vmcnt(16)\n\ts_barrier" ::: "memory");
    STAMP(3);
    issue(4);
    asm volatile("s_waitcnt vmcnt(16)\n\ts_barrier" ::: "memory");
    STAMP(4);
    issue(5);
    asm volatile("s_waitcnt vmcnt(16)\n\ts_barrier" ::: "memory");
    STAMP(5);
    issue(6);
    asm volatile("s_waitcnt vmcnt(16)\n\ts_barrier" ::: "memory");
    STAMP(6);
    asm volatile("s_waitcnt vmcnt(8)\n\ts_barrier" ::: "memory");
    STAMP(7);
    asm volatile("s_waitcnt vmcnt(0)\n\ts_barrier" ::: "memory");
    STAMP(8);
    if (g >= 7 && g <= 10) {
      const int m0 = 128 * (g - 7);
      for (int hd = (m0 + 27 - 27) / 28 + w; hd < 16 && 28 * hd + 27 < m0 + 128; hd += 4) {
        if (28 * hd + 27 >= m0) {
          const unsigned pv = (lane & 3) == 0 ? 0x3F803F80u : 0u;
          st16<AUX1>(ws + WS_KV + (size_t)(hd * 64 + 4 * t + (lane >> 4)) * 4096 + 2048 + (size_t)((lane >> 2) & 3) * 512 + (size_t)(28 + (lane & 3)) * 16, (u32x4){pv, pv, pv, pv});
        }
      }
    }
    STAMP(11); STAMP_OUT(0, w8);
    ENDPGM();
  }
  const int wr_ = w & 1, wc = w >> 1;
  f32x16 acc[2][2] = {};
  const int q = (lane & 15) >> 2, p = lane & 3, G = (lane >> 4) & 1;
  const unsigned xa = l0 + wr_ * 8192 + r * 128 + ((hh ^ ((r >> 1) & 7)) * 16);
  const unsigned wb = l0 + 16384 + wc * 8192 + (8 * hh + q) * 128 + (q >> 1) * 64;
  const int c0 = 128 * g + 64 * wc;
  if (g < 7) {
    k1_compute<1>(xa, wb + (4 * (p & 1) + 2 * G + (p >> 1)) * 8, acc K1_STAMP_PASS);
    STAMP(10);
    const bool isq = c0 < 448;
    const float sc = isq ? 0.1889822365046136f * 1.4426950408889634f : 1.f;
    const unsigned padv = isq ? 0u : 0x3F80u;
    char* base = ws + (isq ? WS_Q : WS_KV) + (size_t)r * 16;
    const unsigned hs = isq ? 131072u : 262144u, ts = isq ? 2048u : 4096u;
#pragma unroll
    for (int ct = 0; ct < 2; ++ct)
#pragma unroll
      for (int e = 0; e < 2; ++e) {
        const int g8 = (c0 - (isq ? 0 : 448)) / 8 + 4 * ct + 2 * hh + e;
        const int hp = (g8 * 37) >> 8, pos = g8 - 7 * hp;
        const unsigned off = (unsigned)(2 * hp + (pos > 3)) * hs + (unsigned)(pos > 3 ? pos - 3 : pos) * 512u;
#pragma unroll
        for (int rt = 0; rt < 2; ++rt) {
          const f32x16 v = acc[rt][ct] * sc;
          u32x4 d = e ? pack8<1>(v) : pack8<0>(v);
          char* dst = base + (size_t)(4 * t + 2 * wr_ + rt) * ts + off;
          if (pos == 3) { st16<AUX1>(dst + hs - 1536, (u32x4){padv, 0u, d[2], d[3]}); d[2] = padv; d[3] = 0u; }
          st16<AUX1>(dst, d);
        }
      }
  } else {
    k1_compute<0>(xa, wb + (4 * G + p) * 8, acc K1_STAMP_PASS);
    STAMP(10);
#pragma unroll
    for (int ct = 0; ct < 2; ++ct) {
      const int c = c0 + 32 * ct + r;
      char* dst; unsigned ts;
      if (c0 < 1344) { const int m = c - 896, hd = (m * 2341) >> 16, d = m - 28 * hd;
        dst = ws + WS_KV + (size_t)(hd * 64 + 4 * t + 2 * wr_) * 4096 + 2048 + (size_t)(d + 32 * hh) * 16; ts = 4096; }
      else { const int tile = 4 * t + 2 * wr_;
        dst = ws + WS_X + (size_t)(((c0 - 1344) >> 5) + ct) * 131072 + (size_t)tile * 2048 + (size_t)lane * 16; ts = 2048; }
#pragma unroll
      for (int rt = 0; rt < 2; ++rt) {
        st16<AUX1>(dst + rt * ts, pack8<0>(acc[rt][ct]));
        st16<AUX1>(dst + rt * ts + 1024, pack8<1>(acc[rt][ct]));
      }
    }
  }
  STAMP(11); STAMP_OUT(0, w8);
}

#define EX(x) x = __builtin_amdgcn_exp2f(x)
DI f32x16 lds_f16(unsigned a) { f32x16 v;
#pragma unroll
  for (int g = 0; g < 4; ++g) { const f32x4 c = *(const LDS_AS f32x4*)(size_t)(a + g * 1024);
#pragma unroll
    for (int e = 0; e < 4; ++e) v[4 * g + e] = c[e]; }
  return v; }
DI void fft_wave(int u, int lane, char* __restrict__ ws, unsigned l0) {
  const bool special = (u == 0) | (u == 224);
  const int cr = u == 224 ? 1 : 2 * u;
  const unsigned vo = lane * 16u;
  { const char* xs = ws + WS_X + (size_t)(cr >> 5) * 131072 + (size_t)(cr & 31) * 16; const char* ts = (const char*)&g_fft; const char* wsrc = ws + WS_WT + (size_t)u * 8192;
    const unsigned xo = (unsigned)(lane & 31) * 2048u + (unsigned)(lane >> 5) * 512u;
#pragma unroll
    for (int i = 0; i < 8; ++i) glds(l0 + i * 1024, xo, xs + (i >> 2) * 16 + ((i >> 1) & 1) * 65536 + (i & 1) * 1024);
#pragma unroll 1
    for (int i = 0; i < 11; ++i) glds4(l0 + 8192 + i * 4096, vo, ts + i * 4096);
    glds4(l0 + 52 * 1024, vo, wsrc); glds4(l0 + 56 * 1024, vo, wsrc + 4096);
#pragma unroll 1
    for (int i = 0; i < 3; ++i) glds4(l0 + (60 + 4 * i) * 1024, vo, ts + (44 + 4 * i) * 1024); }
  unsigned L = l0 + vo, L2 = L + 49152; PIN(L); PIN(L2);
#define FR(i) ((i) < 48 ? lds_b128(L + (i) * 1024) : lds_b128(L2 + ((i) - 48) * 1024))
  asm volatile("s_waitcnt vmcnt(44)" ::: "memory");
  u32x4 ar[4], ai[4];
  const unsigned zmask = special ? 0u : ~0u;
  const f32x16 twc = lds_f16(L + 20 * 1024), tws = lds_f16(L + 24 * 1024);
  {
    bf16x8 z[8], T[12];
#pragma unroll
    for (int i = 0; i < 8; ++i) z[i] = FR(i);
#pragma unroll
    for (int i = 0; i < 12; ++i) T[i] = FR(8 + i);
    SBAR();
#pragma unroll
    for (int i = 4; i < 8; ++i) z[i] = asbf(__builtin_bit_cast(u32x4, z[i]) & zmask);
    f32x16 re[2] = {}, im[2] = {};
#pragma unroll
    for (int rt = 0; rt < 2; ++rt)
#pragma unroll
      for (int s = 0; s < 2; ++s) {
        re[rt] = MFMA(z[rt * 2 + s], T[rt * 6 + s], re[rt]); im[rt] = MFMA(z[4 + rt * 2 + s], T[rt * 6 + s], im[rt]);
        re[rt] = MFMA(z[4 + rt * 2 + s], T[rt * 6 + 2 + s], re[rt]); im[rt] = MFMA(z[rt * 2 + s], T[rt * 6 + 4 + s], im[rt]);
      }
#pragma unroll
    for (int rt = 0; rt < 2; ++rt) {
      const f32x16 r2 = re[rt] * twc + im[rt] * tws, i2 = im[rt] * twc - re[rt] * tws;
      ar[rt * 2] = pack8<0>(r2); ar[rt * 2 + 1] = pack8<1>(r2); ai[rt * 2] = pack8<0>(i2); ai[rt * 2 + 1] = pack8<1>(i2);
    }
    SBAR();
  }
  asm volatile("s_waitcnt vmcnt(12)" ::: "memory");
  u32x4 ur[4], ui[4];
  bf16x8 T64[24];
#pragma unroll
  for (int i = 0; i < 24; ++i) T64[i] = FR(28 + i);
#pragma unroll
  for (int ft = 0; ft < 2; ++ft) {
    u32x4 wv[4];
#pragma unroll
    for (int g = 0; g < 4; ++g) wv[g] = __builtin_bit_cast(u32x4, FR(52 + ft * 4 + g));
    SBAR();
    f32x16 re = {}, im = {};
#pragma unroll
    for (int ks = 0; ks < 4; ++ks) {
      re = MFMA(T64[ft * 12 + ks], asbf(ar[ks]), re); im = MFMA(T64[ft * 12 + ks], asbf(ai[ks]), im);
      re = MFMA(T64[ft * 12 + 4 + ks], asbf(ai[ks]), re); im = MFMA(T64[ft * 12 + 8 + ks], asbf(ar[ks]), im);
    }
    f32x16 wr_, wi_;
#pragma unroll
    for (int g = 0; g < 4; ++g)
#pragma unroll
      for (int e = 0; e < 4; ++e) { wr_[4 * g + e] = __uint_as_float(wv[g][e] << 16); wi_[4 * g + e] = __uint_as_float(wv[g][e] & 0xffff0000u); }
    const f32x16 r2 = re * wr_ - im * wi_, i2 = re * wi_ + im * wr_;
    ur[ft * 2] = pack8<0>(r2); ur[ft * 2 + 1] = pack8<1>(r2); ui[ft * 2] = pack8<0>(i2); ui[ft * 2 + 1] = pack8<1>(i2);
    SBAR();
  }
  asm volatile("s_waitcnt vmcnt(0)" ::: "memory");
  const float osc = special ? 0.5f : 1.f;
#pragma unroll
  for (int ct = 0; ct < 2; ++ct) {
    bf16x8 T[6];
#pragma unroll
    for (int i = 0; i < 6; ++i) T[i] = FR(60 + ct * 6 + i);
    SBAR();
    f32x16 re = {}, im = {};
#pragma unroll
    for (int ks = 0; ks < 4; ++ks) {
      re = MFMA(asbf(ur[ks]), T64[ct * 12 + ks], re); im = MFMA(asbf(ui[ks]), T64[ct * 12 + ks], im);
      re = MFMA(asbf(ui[ks]), T64[ct * 12 + 8 + ks], re); im = MFMA(asbf(ur[ks]), T64[ct * 12 + 4 + ks], im);
    }
    const f32x16 r2 = re * twc - im * tws, i2 = im * twc + re * tws;
    const u32x4 br[2] = {pack8<0>(r2), pack8<1>(r2)}, bi[2] = {pack8<0>(i2), pack8<1>(i2)};
    f32x16 yr = {}, yi = {};
#pragma unroll
    for (int s = 0; s < 2; ++s) {
      yr = MFMA(T[s], asbf(br[s]), yr); yi = MFMA(T[s], asbf(bi[s]), yi);
      yr = MFMA(T[4 + s], asbf(bi[s]), yr); yi = MFMA(T[2 + s], asbf(br[s]), yi);
    }
    yr *= osc;
    u32x4* yd = (u32x4*)(ws + WS_Y + (size_t)cr * 4096 + (size_t)ct * 2048 + (size_t)(lane & 31) * 64 + (size_t)(lane >> 5) * 32);
    st16<AUX2>(yd, pack8<0>(yr)); st16<AUX2>(yd + 1, pack8<1>(yr));
    if (!special) { st16<AUX2>(yd + 256, pack8<0>(yi)); st16<AUX2>(yd + 257, pack8<1>(yi)); }
    SBAR();
  }
}

DI void attn_slow(int h, int tile, int lane, char* __restrict__ ws) {
  const int hh = lane >> 5;
  const u32x4* qp = (const u32x4*)(ws + WS_Q + (size_t)(h * 64 + tile) * 2048) + lane;
  const u32x4 q0 = qp[0], q1 = qp[64];
  const u32x4* kv = (const u32x4*)(ws + WS_KV + (size_t)h * 262144) + lane;
  float m = -INFINITY;
#pragma unroll 1
  for (int i = 0; i < 64; ++i) {
    f32x16 s = {}; s = MFMA(asbf(kv[i * 256]), asbf(q0), s); s = MFMA(asbf(kv[i * 256 + 64]), asbf(q1), s);
    m = fmaxf(m, max16(s));
  }
  m = halfmax(m);
  f32x16 o = {};
#pragma unroll 1
  for (int i = 0; i < 64; ++i) {
    f32x16 s = {}; s = MFMA(asbf(kv[i * 256]), asbf(q0), s); s = MFMA(asbf(kv[i * 256 + 64]), asbf(q1), s);
#pragma unroll
    for (int j = 0; j < 16; ++j) s[j] = __builtin_amdgcn_exp2f(s[j] - m);
    o = MFMA(asbf(kv[i * 256 + 128]), asbf(pack8<0>(s)), o); o = MFMA(asbf(kv[i * 256 + 192]), asbf(pack8<1>(s)), o);
  }
  auto rr = __builtin_amdgcn_permlane32_swap(__float_as_uint(o[12]), __float_as_uint(o[12]), false, false);
  const float rl = 1.f / __uint_as_float(rr[1]);
  o *= rl;
  u32x4 o0 = pack8<0>(o), o1 = pack8<1>(o);
  if (hh == 1) { o1[2] = 0; o1[3] = 0; }
  u32x4* od = (u32x4*)(ws + WS_O + (size_t)(tile * 16 + h) * 2048) + lane;
  od[0] = o0; od[64] = o1;
}

#define FFT_PRIO 3
#define FFT_SLEEP 8
#define N0 13
#define N1 17
#define N2 17
__global__ __launch_bounds__(320, 1) void k2_kernel(const char* __restrict__ wo, char* __restrict__ ws) {
  __shared__ __attribute__((aligned(1024))) char lds[73728 + 49152 + 256];
  const int tid = threadIdx.x, lane = tid & 63, hh = lane >> 5;
  const int w = __builtin_amdgcn_readfirstlane(tid >> 6);
  const int b = blockIdx.x, xcd = b & 7, bi = b >> 3;
  STAMP_DECL; STAMP(0);
  if (w == 4) {
    __builtin_amdgcn_s_sleep(FFT_SLEEP);
    __builtin_amdgcn_s_setprio(FFT_PRIO);
    const int u = 32 * xcd + bi;
    { const unsigned o0 = (bi * 98 + lane) * 128u, o1 = (bi * 98 + 34 + lane) * 128u, lz = (unsigned)(size_t)lds + 73728 + 49152;
      touch(lz, o0, wo); touch(lz, o1, wo); touch(lz, o0, &g_gtab); touch(lz, o1, &g_gtab); }
    if (u < 225) fft_wave(u, lane, ws, (unsigned)(size_t)lds);
    STAMP(11); STAMP_OUT(1, w);
    ENDPGM();
  }
  const int h = 2 * xcd + (bi & 1), t = bi >> 1;
  const int t0 = w == 0 ? 0 : w == 1 ? N0 : w == 2 ? N0 + N1 : N0 + N1 + N2;
  const int t1 = w == 0 ? N0 : w == 1 ? N0 + N1 : w == 2 ? N0 + N1 + N2 : 64;
  const int n = t1 - t0;
  int tc = t0 + ((t * n) >> 4);
  auto nxt = [&](int v) __attribute__((always_inline)) { return v + 1 == t1 ? t0 : v + 1; };
  const auto kvr = __builtin_amdgcn_make_buffer_rsrc((void*)(ws + WS_KV + (size_t)h * 262144), 0, 262144, 0x00020000);
  const unsigned vo = lane * 16u;
#define KV(tile, idx) __builtin_bit_cast(u32x4, __builtin_amdgcn_raw_buffer_load_b128(kvr, vo + (idx) * 1024, (tile) * 4096, 0))
  const u32x4* qp = (const u32x4*)(ws + WS_Q + (size_t)(h * 64 + 4 * t) * 2048) + lane;
  const u32x4 kr0 = KV(4 * t, 0), kr1 = KV(4 * t, 1);
  u32x4 qa0 = qp[0], qa1 = qp[64], qb0 = qp[128], qb1 = qp[192], qc0 = qp[256], qc1 = qp[320], qd0 = qp[384], qd1 = qp[448];
  SBAR();
  u32x4 kA0 = KV(tc, 0), kA1 = KV(tc, 1); SBAR();
  u32x4 vA0 = KV(tc, 2); SBAR();
  u32x4 vA1 = KV(tc, 3); SBAR();
  int tv = nxt(tc);
  u32x4 kB0 = KV(tv, 0); SBAR(); u32x4 kB1 = KV(tv, 1); SBAR();
  u32x4 vB0 = KV(tv, 2), vB1 = {0, 0, 0, 0}; SBAR();
  tc = tv;
  f32x16 oa = {}, ob = {}, oc = {}, od = {}, sa, sb;
  const f32x16 zero = {};
  u32x4 p0, p1 = {0, 0, 0, 0};
  PIN(oa); PIN(ob); PIN(oc); PIN(od); PIN(p1); PIN(vB1); SBAR();
  {
    const bool selA = (h & 1) && hh == 0, selB = !(h & 1) && hh == 1;
    f32x16 s0 = MFMA(asbf(kr0), asbf(qa0), zero); s0 = MFMA(asbf(kr1), asbf(qa1), s0);
    f32x16 s1 = MFMA(asbf(kr0), asbf(qb0), zero); s1 = MFMA(asbf(kr1), asbf(qb1), s1);
    f32x16 s2 = MFMA(asbf(kr0), asbf(qc0), zero); s2 = MFMA(asbf(kr1), asbf(qc1), s2);
    f32x16 s3 = MFMA(asbf(kr0), asbf(qd0), zero); s3 = MFMA(asbf(kr1), asbf(qd1), s3);
#define REF(S, Q0, Q1) do { const unsigned nm = pk(-max16(S), 0.f); Q0[0] = selA ? nm : Q0[0]; Q1[2] = selB ? nm : Q1[2]; } while (0)
    REF(s0, qa0, qa1);
    sa = MFMA(asbf(kA0), asbf(qa0), zero); sa = MFMA(asbf(kA1), asbf(qa1), sa);
    REF(s1, qb0, qb1); REF(s2, qc0, qc1); REF(s3, qd0, qd1);
  }
  sb = zero;
  PIN(sa); STAMP(1); CSTAMP(8);
#define SLOT(S, N, KN0, KN1, QN0, QN1, OP, VP, OC, VC, LD1, LD2)                                                              \
  do {                                                                                                                        \
    SBAR();                                                                                                                   \
    EX(S[0]); EX(S[1]); p1[2] = pk(N[12], N[13]); EX(S[2]); EX(S[3]); p1[3] = pk(N[14], N[15]); PIN(S); PIN(p1); SBAR();      \
    N = MFMA(asbf(KN0), asbf(QN0), zero); SBAR();                                                                             \
    EX(S[4]); EX(S[5]); p0[0] = pk(S[0], S[1]); EX(S[6]); EX(S[7]); p0[1] = pk(S[2], S[3]); PIN(S); PIN(p0); SBAR();          \
    N = MFMA(asbf(KN1), asbf(QN1), N); SBAR();                                                                                \
    LD1;                                                                                                                      \
    EX(S[8]); EX(S[9]); p0[2] = pk(S[4], S[5]); EX(S[10]); EX(S[11]); p0[3] = pk(S[6], S[7]); PIN(S); PIN(p0); SBAR();        \
    OP = MFMA(asbf(VP), asbf(p1), OP); SBAR();                                                                                \
    EX(S[12]); EX(S[13]); p1[0] = pk(S[8], S[9]); EX(S[14]); EX(S[15]); p1[1] = pk(S[10], S[11]); PIN(S); PIN(p1); SBAR();    \
    OC = MFMA(asbf(VC), asbf(p0), OC); SBAR();                                                                                \
    LD2;                                                                                                                      \
  } while (0)
  for (int i = 1; i < n; i += 2) {
    const int tn = nxt(tc);
    SLOT(sa, sb, kA0, kA1, qb0, qb1, od, vB1, oa, vA0, (void)0, vB1 = KV(tv, 3));
    SLOT(sb, sa, kA0, kA1, qc0, qc1, oa, vA1, ob, vA0, (void)0, (void)0);
    SLOT(sa, sb, kA0, kA1, qd0, qd1, ob, vA1, oc, vA0, (kA0 = KV(tn, 0), kA1 = KV(tn, 1)), (void)0);
    SLOT(sb, sa, kB0, kB1, qa0, qa1, oc, vA1, od, vA0, (void)0, vA0 = KV(tn, 2));
    tc = nxt(tn);
    SLOT(sa, sb, kB0, kB1, qb0, qb1, od, vA1, oa, vB0, (void)0, vA1 = KV(tn, 3));
    SLOT(sb, sa, kB0, kB1, qc0, qc1, oa, vB1, ob, vB0, (void)0, (void)0);
    SLOT(sa, sb, kB0, kB1, qd0, qd1, ob, vB1, oc, vB0, (kB0 = KV(tc, 0), kB1 = KV(tc, 1)), (void)0);
    SLOT(sb, sa, kA0, kA1, qa0, qa1, oc, vB1, od, vB0, (void)0, vB0 = KV(tc, 2));
    tv = tc;
  }
  SLOT(sa, sb, kA0, kA1, qb0, qb1, od, vB1, oa, vA0, (void)0, (void)0);
  SLOT(sb, sa, kA0, kA1, qc0, qc1, oa, vA1, ob, vA0, (void)0, (void)0);
  SLOT(sa, sb, kA0, kA1, qd0, qd1, ob, vA1, oc, vA0, (void)0, (void)0);
  SLOT(sb, sa, kA0, kA1, qa0, qa1, oc, vA1, od, vA0, (void)0, (void)0);
  p1[2] = pk(sb[12], sb[13]); p1[3] = pk(sb[14], sb[15]);
  od = MFMA(asbf(vA1), asbf(p1), od);
  PIN(oa); PIN(od); STAMP(2); CSTAMP(9);
  f32x4* cw = (f32x4*)(lds + 73728) + lane;
#define PUT(O, DST) do { if (w != DST) { const int slot = DST * 3 + (w > DST ? w - 1 : w); _Pragma("unroll") for (int i = 0; i < 4; ++i) \
      cw[slot * 256 + i * 64] = (f32x4){O[4 * i], O[4 * i + 1], O[4 * i + 2], O[4 * i + 3]}; } } while (0)
  PUT(oa, 0); PUT(ob, 1); PUT(oc, 2); PUT(od, 3);
  f32x16 o = w == 0 ? oa : w == 1 ? ob : w == 2 ? oc : od;
  STAMP(3);
  __syncthreads();
  STAMP(4);
#pragma unroll
  for (int j = 0; j < 3; ++j)
#pragma unroll
    for (int i = 0; i < 4; ++i) { const f32x4 v = cw[(w * 3 + j) * 256 + i * 64]; o[4 * i] += v[0]; o[4 * i + 1] += v[1]; o[4 * i + 2] += v[2]; o[4 * i + 3] += v[3]; }
  auto rr = __builtin_amdgcn_permlane32_swap(__float_as_uint(o[12]), __float_as_uint(o[12]), false, false);
  const float l = __uint_as_float(rr[1]);
  float amax = 0.f, nanchk = 0.f;
#pragma unroll
  for (int i = 0; i < 16; i += 2) { amax = fmaxf(fmaxf(amax, fabsf(o[i])), fabsf(o[i + 1])); nanchk += o[i] + o[i + 1]; }
  const bool ok = (l >= 1.0e-12f) && (l <= 1.0e12f) && (amax <= 1.0e37f) && (nanchk == nanchk);
  if (__builtin_expect(!__all(ok), 0)) {
    attn_slow(h, 4 * t + w, lane, ws);
    return;
  }
  o *= 1.f / l;
  u32x4 o0 = pack8<0>(o), o1 = pack8<1>(o);
  if (hh == 1) { o1[2] = 0; o1[3] = 0; }
  u32x4* odst = (u32x4*)(ws + WS_O + (size_t)((4 * t + w) * 16 + h) * 2048) + lane;
  st16<AUX2>(odst, o0); st16<AUX2>(odst + 64, o1);
  STAMP(11); STAMP_OUT(1, w);
}

#define GLD(dst, voff, sb, imm) asm volatile("global_load_dwordx4 %0, %1, %2 offset:" #imm : "=v"(dst) : "v"(voff), "s"(sb) : "memory")
__global__ __launch_bounds__(512, 2) void k3_kernel(const char* __restrict__ wo, const char* __restrict__ ws, char* __restrict__ out) {
  __shared__ __attribute__((aligned(1024))) char lds[2 * 57344 + 16384];
  const int tid = threadIdx.x, lane = tid & 63, r = lane & 31, hh = lane >> 5;
  const int w8 = __builtin_amdgcn_readfirstlane(tid >> 6), w = w8 & 3;
  const int b = blockIdx.x, rb = 4 * (b & 7) + (b >> 3) / 7, cb = (b >> 3) % 7;
  const unsigned l0 = (unsigned)(size_t)lds;
  STAMP_DECL; STAMP(0);
  if (w8 < 4) {
    const unsigned sw = (unsigned)(((lane & 7) ^ (4 * ((lane >> 4) & 1))) * 16);
    { const unsigned so = (unsigned)(lane >> 3) * 896u + sw; const char* sb = wo + cb * 128 + (size_t)w * 112 * 896;
#pragma unroll
      for (int j = 0; j < 14; ++j) glds(l0 + 57344 + w * 14336 + j * 1024, so, sb + (size_t)j * 8 * 896); }
    asm volatile("s_barrier" ::: "memory");
    const unsigned so = (unsigned)(lane >> 3) * 4096u + sw;
    const char* sb = ws + WS_Y + rb * 128 + (size_t)w * 8 * 4096;
    auto issue = [&](int st) __attribute__((always_inline)) {
#pragma unroll
      for (int j = 0; j < 4; ++j) { const int pc = (j == 3 && w >= 2) ? 2 : j; glds(l0 + st * 14336 + (pc * 4 + w) * 1024, so, sb + (size_t)(st * 112 + pc * 32) * 4096); } };
    issue(0); issue(1);
    asm volatile("s_waitcnt vmcnt(4)\n\ts_barrier" ::: "memory");
    issue(2);
    asm volatile("s_waitcnt vmcnt(4)\n\ts_barrier" ::: "memory");
    issue(3);
    asm volatile("s_waitcnt vmcnt(4)\n\ts_barrier" ::: "memory");
    asm volatile("s_waitcnt vmcnt(0)\n\ts_barrier" ::: "memory");
    STAMP(11); STAMP_OUT(2, w8);
    ENDPGM();
  }
  const bool isb = w >= 2; const int dt = w & 1;
  const unsigned vo = lane * 16u;
  u32x4 f[4][8];
  const int frt = dt, fct = isb ? 1 - dt : dt;
  float g;
  f32x16 acc0 = {}, acc1 = {};
  const int q = (lane & 15) >> 2, p = lane & 3, G = (lane >> 4) & 1;
#define K3_WAIT(ST, N) asm volatile("s_waitcnt vmcnt(" #N ")\n\ts_barrier" : "+v"(f[ST][0]), "+v"(f[ST][1]), "+v"(f[ST][2]), "+v"(f[ST][3]), "+v"(f[ST][4]), \
               "+v"(f[ST][5]), "+v"(f[ST][6]), "+v"(f[ST][7])::"memory")
  if (!isb) {
    const char* db = (const char*)&g_gtab + (size_t)(2 * cb + dt) * 28672;
#pragma unroll
    for (int st = 0; st < 4; ++st) { const char* d0 = db + st * 7168; const char* d1 = d0 + 4096;
      GLD(f[st][0], vo, d0, 0); GLD(f[st][1], vo, d0, 1024); GLD(f[st][2], vo, d0, 2048); GLD(f[st][3], vo, d0, 3072);
      GLD(f[st][4], vo, d1, 0); GLD(f[st][5], vo, d1, 1024); GLD(f[st][6], vo, d1, 2048); f[st][7] = (u32x4){0, 0, 0, 0}; }
    asm volatile("global_load_dword %0, %1, %2" : "=v"(g) : "v"(r * 4u), "s"(ws + WS_G + (size_t)(rb * 64 + frt * 32) * 4) : "memory");
    asm volatile("s_barrier" ::: "memory");
    STAMP(1);
    unsigned y0 = l0 + (8 * hh + q) * 128 + ((q >> 1) * 64) + G * 32 + p * 8, y1 = y0 ^ 64; PIN(y0); PIN(y1);
    bf16x8 a0, a1, b0, b1;
#define K3_RA(D0, D1, KS) do { D0 = lds_tr2(y0 + (KS) * 2048, y0 + (KS) * 2048 + 512); D1 = lds_tr2(y1 + (KS) * 2048, y1 + (KS) * 2048 + 512); SBAR(); } while (0)
#define K3_MA(D0, D1, F) do { acc0 = MFMA(asbf(F), D0, acc0); acc1 = MFMA(asbf(F), D1, acc1); SBAR(); } while (0)
#define K3_STA(ST) K3_WAIT(ST, 0); STAMP(2 + ST);                                                                          \
    K3_RA(a0, a1, ST * 7); K3_RA(b0, b1, ST * 7 + 1); K3_MA(a0, a1, f[ST][0]); K3_RA(a0, a1, ST * 7 + 2); K3_MA(b0, b1, f[ST][1]); \
    K3_RA(b0, b1, ST * 7 + 3); K3_MA(a0, a1, f[ST][2]); K3_RA(a0, a1, ST * 7 + 4); K3_MA(b0, b1, f[ST][3]);               \
    K3_RA(b0, b1, ST * 7 + 5); K3_MA(a0, a1, f[ST][4]); K3_RA(a0, a1, ST * 7 + 6); K3_MA(b0, b1, f[ST][5]); K3_MA(a0, a1, f[ST][6]);
    K3_STA(0) K3_STA(1) K3_STA(2) K3_STA(3)
  } else {
    { const unsigned ln = cb * 74 + dt * 37 + (lane < 37 ? lane : 0);
      touch(l0 + 114688 + w * 256, (ln < 512 ? ln : 0) * 128u, ws + WS_O + (size_t)rb * 65536);
      touch(l0 + 114688 + w * 256, (unsigned)(cb * 64 + dt * 32 + r) * 4096u, ws + WS_Y + rb * 128); }
    asm volatile("global_load_dword %0, %1, %2" : "=v"(g) : "v"(r * 4u), "s"(ws + WS_G + (size_t)(rb * 64 + frt * 32) * 4) : "memory");
    asm volatile("s_barrier" ::: "memory");
    STAMP(1);
    const char* db = ws + WS_O + (size_t)(2 * rb + dt) * 32768;
#define K3_LOAD(I) do { const char* d0 = db + I * 8192; const char* d1 = d0 + 4096;                                              \
    GLD(f[I][0], vo, d0, 0); GLD(f[I][1], vo, d0, 1024); GLD(f[I][2], vo, d0, 2048); GLD(f[I][3], vo, d0, 3072);                  \
    GLD(f[I][4], vo, d1, 0); GLD(f[I][5], vo, d1, 1024); GLD(f[I][6], vo, d1, 2048); GLD(f[I][7], vo, d1, 3072); } while (0)
    K3_LOAD(0); K3_LOAD(1);
    unsigned w0 = l0 + 57344 + (4 * hh + q) * 128 + ((q >> 1) * 64) + (4 * (p & 1) + 2 * G + (p >> 1)) * 8, w1 = w0 ^ 64;
    unsigned w2 = w0 - hh * 512, w3 = w1 - hh * 512;
    PIN(w0); PIN(w1); PIN(w2); PIN(w3);
    bf16x8 a0, a1, b0, b1;
#define K3_OFF(J) (((J) >> 1) * 3584 + ((J) & 1) * 2048)
#define K3_RB(D0, D1, J) do { D0 = lds_tr2(w0 + K3_OFF(J), (((J) & 1) ? w2 : w0) + K3_OFF(J) + 1024); D1 = lds_tr2(w1 + K3_OFF(J), (((J) & 1) ? w3 : w1) + K3_OFF(J) + 1024); SBAR(); } while (0)
#define K3_MB(D0, D1, F) do { acc0 = MFMA(D0, asbf(F), acc0); acc1 = MFMA(D1, asbf(F), acc1); SBAR(); } while (0)
#define K3_STB(ST, N, NEXT) K3_WAIT(ST, N); STAMP(2 + ST);                                                                  \
    K3_RB(a0, a1, ST * 8); K3_RB(b0, b1, ST * 8 + 1); K3_MB(a0, a1, f[ST][0]); K3_RB(a0, a1, ST * 8 + 2); K3_MB(b0, b1, f[ST][1]); \
    K3_RB(b0, b1, ST * 8 + 3); K3_MB(a0, a1, f[ST][2]); K3_RB(a0, a1, ST * 8 + 4); K3_MB(b0, b1, f[ST][3]);               \
    K3_RB(b0, b1, ST * 8 + 5); K3_MB(a0, a1, f[ST][4]); K3_RB(a0, a1, ST * 8 + 6); K3_MB(b0, b1, f[ST][5]);               \
    K3_RB(b0, b1, ST * 8 + 7); K3_MB(a0, a1, f[ST][6]); NEXT; K3_MB(b0, b1, f[ST][7]);
    K3_STB(0, 8, K3_LOAD(2)) K3_STB(1, 8, K3_LOAD(3)) K3_STB(2, 8, (void)0) K3_STB(3, 0, (void)0)
  }
  asm volatile("" : "+v"(g));
  PIN(acc0); PIN(acc1); STAMP(6);
  const bool keep1 = isb ? (dt == 0) : (dt == 1);
  const f32x16 mine = keep1 ? acc1 : acc0, send = keep1 ? acc0 : acc1;
  f32x4* ex = (f32x4*)(lds + 114688) + lane;
  const int to = isb ? dt : 3 - dt;
#pragma unroll
  for (int i = 0; i < 4; ++i) ex[to * 256 + i * 64] = (f32x4){send[4 * i], send[4 * i + 1], send[4 * i + 2], send[4 * i + 3]};
  const float ga = g * (2.0f / (2048.f * 448.f)), g1 = rbf(1.f - g);
  const f32x16 part = mine * (isb ? g1 : ga);
  const float go = isb ? ga : g1;
  __syncthreads();
  f32x16 res;
#pragma unroll
  for (int i = 0; i < 4; ++i) { const f32x4 v = ex[w * 256 + i * 64];
#pragma unroll
    for (int e = 0; e < 4; ++e) res[4 * i + e] = fmaf(v[e], go, part[4 * i + e]); }
  const int srow = 64 * r + 2 * rb + frt;
  u32x4* od = (u32x4*)(out + (size_t)srow * 896 + (size_t)(cb * 64 + fct * 32 + 16 * hh) * 2);
  st16<AUX3>(od, pack8<0>(res)); st16<AUX3>(od + 1, pack8<1>(res));
  STAMP(11); STAMP_OUT(2, w8);
}

extern "C" void kernel_launch(void* const* d_in, const int* in_sizes, int n_in, void* d_out, int out_size, void* d_ws, size_t ws_size, hipStream_t stream) {
  char* ws = (char*)d_ws;
  k1_kernel<<<256, 512, 0, stream>>>((const char*)d_in[0], (const char*)d_in[4], (const char*)d_in[6], (const char*)d_in[1], (const char*)d_in[2], ws);
  k2_kernel<<<256, 320, 0, stream>>>((const char*)d_in[5], ws);
  k3_kernel<<<224, 512, 0, stream>>>((const char*)d_in[5], ws, (char*)d_out);
}
```

```cpp
#include <hip/hip_runtime.h>
#include <stdint.h>

typedef short bf16x8 __attribute__((ext_vector_type(8)));
typedef short s16x4 __attribute__((ext_vector_type(4)));
typedef float f32x16 __attribute__((ext_vector_type(16)));
typedef float f32x4 __attribute__((ext_vector_type(4)));
typedef float f32x2 __attribute__((ext_vector_type(2)));
typedef unsigned u32x4 __attribute__((ext_vector_type(4)));
typedef __bf16 bf16x2_t __attribute__((ext_vector_type(2)));
#define K1_MAP 1
#define DI __device__ __forceinline__
#define LDS_AS __attribute__((address_space(3)))

namespace tab {
constexpr double PI = 3.14159265358979323846264338327950288;
constexpr double tsin(double x) { double x2 = x * x, t = x, s = x; for (int i = 1; i <= 10; ++i) { t *= -x2 / ((2 * i) * (2 * i + 1)); s += t; } return s; }
constexpr double tcos(double x) { double x2 = x * x, t = 1, s = 1; for (int i = 1; i <= 10; ++i) { t *= -x2 / ((2 * i - 1) * (2 * i)); s += t; } return s; }
constexpr double cosr(int m, int N) {
  m %= N; if (m < 0) m += N; if (2 * m > N) m = N - m;
  bool neg = false; if (4 * m > N) { m = N / 2 - m; neg = true; }
  double r = (8 * m > N) ? tsin(2 * PI * (N / 4 - m) / N) : tcos(2 * PI * m / N); return neg ? -r : r; }
constexpr double sinr(int m, int N) { return cosr(m - N / 4, N); }
constexpr uint32_t f2bf(double d) { float f = (float)d; uint32_t u = __builtin_bit_cast(uint32_t, f); u += 0x7fffu + ((u >> 16) & 1u); return u >> 16; }
constexpr int crow(int r, int hh) { return (r & 3) + 8 * (r >> 2) + 4 * hh; }
constexpr int krow(int s, int hh, int j) { return 16 * s + 8 * (j >> 2) + 4 * hh + (j & 3); }
constexpr int sigma(int r) { return 16 * ((r >> 2) & 1) + 4 * (r >> 3) + (r & 3); }
struct Base448 { uint16_t c[448], ns[448]; };
constexpr Base448 mk448() { Base448 b{}; for (int i = 0; i < 448; ++i) { b.c[i] = f2bf(cosr(i, 448)); b.ns[i] = f2bf(-sinr(i, 448)); } return b; }
constexpr Base448 B448 = mk448();
constexpr uint32_t Fval(int j, int c) { return c == 1 ? B448.c[(j * 224) % 448] : ((c & 1) ? B448.ns[(j * (c >> 1)) % 448] : B448.c[(j * (c >> 1)) % 448]); }
struct Tile { uint32_t v[28 * 64 * 4]; };
constexpr Tile mkF(int jt) { Tile t{}; for (int j = 0; j < 32; ++j) for (int c = 0; c < 224; ++c) t.v[j * 224 + c] = Fval(jt * 32 + j, 2 * c) | (Fval(jt * 32 + j, 2 * c + 1) << 16); return t; }
constexpr Tile mkG(int jt) { Tile t{}; for (int ks = 0; ks < 28; ++ks) for (int l = 0; l < 64; ++l) { int j = jt * 32 + sigma(l & 31), c0 = ks * 16 + 8 * (l >> 5);
  for (int e = 0; e < 4; ++e) t.v[(ks * 64 + l) * 4 + e] = Fval(j, c0 + 2 * e) | (Fval(j, c0 + 2 * e + 1) << 16); } return t; }
template <int T> constexpr Tile FT = mkF(T);
template <int T> constexpr Tile GT = mkG(T);
struct Tab14 { Tile t[14]; };
struct Fft { uint32_t t1[12][256]; float tw[8][256]; uint32_t t64[24][256]; uint32_t t4[12][256]; };
struct Base64 { uint16_t v[3][64]; };
constexpr Base64 mk64() { Base64 b{}; for (int i = 0; i < 64; ++i) { b.v[0][i] = f2bf(cosr(i, 64)); b.v[1][i] = f2bf(sinr(i, 64)); b.v[2][i] = f2bf(-sinr(i, 64)); } return b; }
constexpr Base64 B64 = mk64();
constexpr uint32_t cs64(int m, int which) { return B64.v[which][m & 63]; }
constexpr Fft mkFft() { Fft f{};
  for (int l = 0; l < 64; ++l) { const int r = l & 31, hh = l >> 5;
    for (int e = 0; e < 4; ++e) {
      for (int t = 0; t < 2; ++t) for (int w = 0; w < 3; ++w) for (int s = 0; s < 2; ++s) {
        const int k0 = krow(s, hh, 2 * e), k1 = krow(s, hh, 2 * e + 1), n1 = sigma(r);
        f.t1[(t * 3 + w) * 2 + s][l * 4 + e] = cs64((2 * k0 + t) * r, w) | (cs64((2 * k1 + t) * r, w) << 16);
        f.t4[(t * 3 + w) * 2 + s][l * 4 + e] = cs64((2 * n1 + t) * k0, w) | (cs64((2 * n1 + t) * k1, w) << 16); }
      for (int t = 0; t < 2; ++t) for (int w = 0; w < 3; ++w) for (int ks = 0; ks < 4; ++ks) {
        const int a = 32 * t + r, b0 = 32 * (ks >> 1) + krow(ks & 1, hh, 2 * e), b1 = 32 * (ks >> 1) + krow(ks & 1, hh, 2 * e + 1);
        f.t64[(t * 3 + w) * 4 + ks][l * 4 + e] = cs64(a * b0, w) | (cs64(a * b1, w) << 16); }
      for (int g = 0; g < 4; ++g) { const int m = crow(4 * g + e, hh) * r; f.tw[g][l * 4 + e] = (float)cosr(m, 2048); f.tw[4 + g][l * 4 + e] = (float)sinr(m, 2048); }
    } }
  return f; }
constexpr Fft FFT = mkFft();
}
#define TAB14(X) {{tab::X<0>, tab::X<1>, tab::X<2>, tab::X<3>, tab::X<4>, tab::X<5>, tab::X<6>, tab::X<7>, tab::X<8>, tab::X<9>, tab::X<10>, tab::X<11>, tab::X<12>, tab::X<13>}}
__device__ __attribute__((aligned(256))) const tab::Tab14 g_ftab = TAB14(FT);
__device__ __attribute__((aligned(256))) const tab::Tab14 g_gtab = TAB14(GT);
__device__ __attribute__((aligned(256))) const tab::Fft g_fft = tab::FFT;

constexpr size_t WS_Q = 0;
constexpr size_t WS_KV = WS_Q + (2u << 20);
constexpr size_t WS_X = WS_KV + (4u << 20);
constexpr size_t WS_WT = WS_X + (2u << 20);
constexpr size_t WS_G = WS_WT + (2u << 20);
constexpr size_t WS_O = WS_G + (1u << 20);
constexpr size_t WS_Y = WS_O + (2u << 20);

#define STAMP_DECL
#define STAMP(i)
#define STAMP_OUT(K, W)
#define CSTAMP(i)
#define MFMA(a, b, c) __builtin_amdgcn_mfma_f32_32x32x16_bf16((a), (b), (c), 0, 0, 0)
#define SBAR() __builtin_amdgcn_sched_barrier(0)
#define PIN(x) asm volatile("" : "+v"(x))
DI unsigned pk(float lo, float hi) { f32x2 v = {lo, hi}; return __builtin_bit_cast(unsigned, __builtin_convertvector(v, bf16x2_t)); }
DI bf16x8 asbf(u32x4 v) { return __builtin_bit_cast(bf16x8, v); }
template <int S> DI u32x4 pack8(const f32x16& a) { return (u32x4){pk(a[8 * S], a[8 * S + 1]), pk(a[8 * S + 2], a[8 * S + 3]), pk(a[8 * S + 4], a[8 * S + 5]), pk(a[8 * S + 6], a[8 * S + 7])}; }
DI float rbf(float v) { return __uint_as_float(pk(v, v) << 16); }
DI void glds(unsigned lds, unsigned voff, const void* sbase) {
  asm volatile("s_mov_b32 m0, %0\n\ts_nop 0\n\tglobal_load_lds_dwordx4 %1, %2" ::"s"(lds), "v"(voff), "s"(sbase) : "memory"); }
DI void glds4(unsigned lds, unsigned voff, const void* sbase) {
  asm volatile("s_mov_b32 m0, %0\n\ts_nop 0\n\tglobal_load_lds_dwordx4 %1, %2\n\tglobal_load_lds_dwordx4 %1, %2 offset:1024\n\t"
               "global_load_lds_dwordx4 %1, %2 offset:2048\n\tglobal_load_lds_dwordx4 %1, %2 offset:3072" ::"s"(lds), "v"(voff), "s"(sbase) : "memory"); }
DI void touch(unsigned lds, unsigned voff, const void* sbase) {
  asm volatile("s_mov_b32 m0, %0\n\ts_nop 0\n\tglobal_load_lds_dword %1, %2" ::"s"(lds), "v"(voff), "s"(sbase) : "memory"); }
#define AUX1 16
#define AUX2 16
#define AUX3 0
typedef unsigned v4u_t __attribute__((ext_vector_type(4)));
template <int AUX> DI void st16(void* p, u32x4 v) {
  if (AUX == 0) *(u32x4*)p = v;
  else if (AUX == 2) asm volatile("global_store_dwordx4 %0, %1, off nt\n\ts_nop 1" ::"v"(p), "v"(v) : "memory");
  else if (AUX == 16) asm volatile("global_store_dwordx4 %0, %1, off sc1\n\ts_nop 1" ::"v"(p), "v"(v) : "memory");
  else asm volatile("global_store_dwordx4 %0, %1, off sc0 sc1\n\ts_nop 1" ::"v"(p), "v"(v) : "memory");
}
#define CODE_LINES 230
#define ENDPGM() __builtin_amdgcn_endpgm()
typedef short v4i16_t __attribute__((ext_vector_type(4)));
DI bf16x8 lds_b128(unsigned a) { return *(const LDS_AS bf16x8*)(size_t)a; }
DI bf16x8 lds_tr2(unsigned a0, unsigned a1) {
  v4i16_t lo = __builtin_amdgcn_ds_read_tr16_b64_v4i16((LDS_AS v4i16_t*)(size_t)a0), hi = __builtin_amdgcn_ds_read_tr16_b64_v4i16((LDS_AS v4i16_t*)(size_t)a1);
  return __builtin_shufflevector(lo, hi, 0, 1, 2, 3, 4, 5, 6, 7); }
DI float halfmax(float m) { auto r = __builtin_amdgcn_permlane32_swap(__float_as_uint(m), __float_as_uint(m), false, false); return fmaxf(__uint_as_float(r[0]), __uint_as_float(r[1])); }
DI float max16(const f32x16& s) { float m = fmaxf(s[0], s[1]);
#pragma unroll
  for (int i = 2; i < 16; i += 2) m = fmaxf(fmaxf(m, s[i]), s[i + 1]);
  return m; }

constexpr int K1_SLOT = 32768;
struct K1Frag { bf16x8 x0, x1, w0, w1; };
struct K1Addr { unsigned x[4], w0, w1; };
DI void k1_read(K1Frag& f, const K1Addr& A, unsigned o, int ks) {
  f.x0 = lds_b128(A.x[ks] + o); f.w0 = lds_tr2(A.w0 + o + ks * 2048, A.w0 + o + ks * 2048 + 512);
  f.x1 = lds_b128(A.x[ks] + o + 4096); f.w1 = lds_tr2(A.w1 + o + ks * 2048, A.w1 + o + ks * 2048 + 512);
}
template <int TR> DI void k1_mma(const K1Frag& f, f32x16 (&acc)[2][2]) {
  if (TR) { acc[0][0] = MFMA(f.w0, f.x0, acc[0][0]); acc[1][0] = MFMA(f.w0, f.x1, acc[1][0]); acc[0][1] = MFMA(f.w1, f.x0, acc[0][1]); acc[1][1] = MFMA(f.w1, f.x1, acc[1][1]); }
  else    { acc[0][0] = MFMA(f.x0, f.w0, acc[0][0]); acc[1][0] = MFMA(f.x1, f.w0, acc[1][0]); acc[0][1] = MFMA(f.x0, f.w1, acc[0][1]); acc[1][1] = MFMA(f.x1, f.w1, acc[1][1]); }
}
#define K1_STAMP_ARG
#define K1_STAMP_PASS
template <int TR> DI void k1_compute(unsigned xa, unsigned wa, f32x16 (&acc)[2][2] K1_STAMP_ARG) {
  K1Addr A;
#pragma unroll
  for (int ks = 0; ks < 4; ++ks) A.x[ks] = xa ^ (ks * 32);
  A.w0 = wa; A.w1 = wa ^ 64;
  K1Frag fa, fb = {};
#define K1_RD(F, KS, M0, M1, M2, M3, G) do {                                                                                     \
    F.x0 = lds_b128(A.x[KS]); SBAR(); M0(G); SBAR();                                                                              \
    F.w0 = lds_tr2(A.w0 + KS * 2048, A.w0 + KS * 2048 + 512); if (KS == 3) { A.w0 += d; PIN(A.w0); } SBAR(); M1(G); SBAR();       \
    F.x1 = lds_b128(A.x[KS] + 4096); A.x[KS] += d; PIN(A.x[KS]); SBAR(); M2(G); SBAR();                                           \
    F.w1 = lds_tr2(A.w1 + KS * 2048, A.w1 + KS * 2048 + 512); if (KS == 3) { A.w1 += d; PIN(A.w1); } SBAR(); M3(G); SBAR(); } while (0)
#define K1_M0(G) acc[0][0] = TR ? MFMA(G.w0, G.x0, acc[0][0]) : MFMA(G.x0, G.w0, acc[0][0])
#define K1_M1(G) acc[1][0] = TR ? MFMA(G.w0, G.x1, acc[1][0]) : MFMA(G.x1, G.w0, acc[1][0])
#define K1_M2(G) acc[0][1] = TR ? MFMA(G.w1, G.x0, acc[0][1]) : MFMA(G.x0, G.w1, acc[0][1])
#define K1_M3(G) acc[1][1] = TR ? MFMA(G.w1, G.x1, acc[1][1]) : MFMA(G.x1, G.w1, acc[1][1])
  __builtin_amdgcn_s_waitcnt(0xC07F);
#pragma unroll 1
  for (int i = 0; i < 7; ++i) {
    asm volatile("s_barrier" ::: "memory");
    const unsigned d = i == 4 ? (unsigned)(-4 * K1_SLOT) : (unsigned)K1_SLOT;
    K1_RD(fa, 0, K1_M0, K1_M1, K1_M2, K1_M3, fb);
    K1_RD(fb, 1, K1_M0, K1_M1, K1_M2, K1_M3, fa);
    K1_RD(fa, 2, K1_M0, K1_M1, K1_M2, K1_M3, fb);
    K1_RD(fb, 3, K1_M0, K1_M1, K1_M2, K1_M3, fa);
  }
  k1_mma<TR>(fb, acc);
  PIN(acc[0][0]); PIN(acc[1][1]); STAMP(2); CSTAMP(9);
}

__global__ __launch_bounds__(512, 2) void k1_kernel(const char* __restrict__ x, const char* __restrict__ wqkv, const char* __restrict__ gw,
                                                     const char* __restrict__ wr, const char* __restrict__ wi, char* __restrict__ ws) {
  __shared__ __attribute__((aligned(1024))) char lds[5 * K1_SLOT];
  const int tid = threadIdx.x, lane = tid & 63, r = lane & 31, hh = lane >> 5;
  const int w8 = __builtin_amdgcn_readfirstlane(tid >> 6), w = w8 & 3;
  const int b = blockIdx.x, xcd = b & 7, bi = b >> 3;
  const unsigned l0 = (unsigned)(size_t)lds;
  STAMP_DECL; STAMP(0);
  if (bi >= 28) {
    const int id = xcd * 4 + (bi - 28), fq = id >> 1, f1h = id & 1;
    { const char* src = (w8 < 4 ? wr : wi) + (size_t)(128 * fq + 32 * w + 16 * f1h) * 450;
      const unsigned lb = l0 + w8 * 8192;
      glds4(lb, lane * 16u, src);
      glds(lb + 4096, lane * 16u, src + 4096); glds(lb + 5120, lane * 16u, src + 5120); glds(lb + 6144, lane * 16u, src + 6144);
      glds(lb + 7168, (lane < 2 ? lane : 0) * 16u, src + 7168); }
    { const int p = 64 * id + 8 * w8 + (lane >> 3);
      const u32x4* xr = (const u32x4*)(x + (size_t)(64 * (p & 31) + (p >> 5)) * 896) + (lane & 7);
      const u32x4* gr = (const u32x4*)gw + (lane & 7);
      u32x4 gx[7], gv[7];
#pragma unroll
      for (int i = 0; i < 7; ++i) { gx[i] = xr[8 * i]; gv[i] = gr[8 * i]; }
      if (bi == 28 && w8 < 7) touch(l0 + 65536 + w8 * 256, (w8 * 64 + lane) * 128u, &g_fft);
      if (bi == 29 && w8 < 5) {
        unsigned long long pc; unsigned rel;
        asm volatile("s_getpc_b64 %0\n\ts_mov_b32 %1, _Z9k1_kernelPKcS0_S0_S0_S0_Pc@rel32@lo+4" : "=s"(pc), "=s"(rel));
        const char* code = (const char*)(pc + (long long)(int)rel);
        const unsigned ln = w8 * 64 + lane;
        touch(l0 + 65536 + w8 * 256, (ln < CODE_LINES ? ln : 0) * 128u, code); }
      float z = 0.f;
#pragma unroll
      for (int i = 0; i < 7; ++i)
#pragma unroll
        for (int e = 0; e < 4; ++e) { z = fmaf(__uint_as_float(gx[i][e] << 16), __uint_as_float(gv[i][e] << 16), z); z = fmaf(__uint_as_float(gx[i][e] & 0xffff0000u), __uint_as_float(gv[i][e] & 0xffff0000u), z); }
      z += __shfl_xor(z, 1); z += __shfl_xor(z, 2); z += __shfl_xor(z, 4);
      if ((lane & 7) == 0) *(float*)(ws + WS_G + (size_t)p * 4) = rbf(1.f / (1.f + __expf(-rbf(z)))); }
    asm volatile("s_waitcnt vmcnt(0)\n\ts_barrier" ::: "memory");
    { const int f1l = tid & 15, k0 = tid >> 4;
      const LDS_AS unsigned short* src = (const LDS_AS unsigned short*)(size_t)(l0 + f1l * 450 + k0 * 2);
      char* dst = ws + WS_WT + (size_t)k0 * 8192 + (size_t)(fq >> 1) * 1024 + (size_t)(32 * (fq & 1) + 16 * f1h + f1l) * 16;
      u32x4 v[8];
#pragma unroll
      for (int it = 0; it < 8; ++it)
#pragma unroll
        for (int e = 0; e < 4; ++e) v[it][e] = (unsigned)src[e * 4096 + it * 32] | ((unsigned)src[16384 + e * 4096 + it * 32] << 16);
#pragma unroll
      for (int it = 0; it < 8; ++it) if (it < 7 || k0 == 0) st16<AUX1>(dst + (size_t)it * 32 * 8192, v[it]);
    }
    STAMP(9); STAMP(11); STAMP_OUT(0, w8);
    ENDPGM();
  }
  const int t = 4 * (xcd >> 1) + (bi & 3), g = 7 * (xcd & 1) + (bi >> 2);
  if (w8 < 4) {
    const int rot = (g + 2 * t) % 7;
    const unsigned xo_e = (unsigned)(lane >> 3) * (64u * 896u) + (unsigned)(((lane & 7) ^ (lane >> 4)) * 16);
    const unsigned xo_o = xo_e ^ 64u;
    const char* xb = x + (size_t)(4 * t + w) * 896;
    const int c0 = 128 * g + 64 * (w >> 1);
    const unsigned rs = c0 < 1344 ? 2688u : 896u;
    const char* cb = (c0 < 1344 ? wqkv + c0 * 2 : (const char*)&g_ftab + (c0 - 1344) * 2) + (size_t)(32 * (w & 1)) * rs;
    const unsigned co = (unsigned)(lane >> 3) * rs + (unsigned)(((lane & 7) ^ (4 * ((lane >> 4) & 1))) * 16);
    auto issue = [&](int i) __attribute__((always_inline)) {
      int c = i + rot; c = c >= 7 ? c - 7 : c;
      const unsigned lb = l0 + (i % 5) * K1_SLOT + w * 4096;
      const char* xc = xb + c * 128; const char* cc = cb + (size_t)(c * 64) * rs;
#pragma unroll
      for (int j = 0; j < 4; ++j) {
        glds(lb + j * 1024, (j & 1) ? xo_o : xo_e, xc + j * (8 * 64 * 896));
        glds(lb + 16384 + j * 1024, co, cc + j * 8 * rs);
      }
    };
    issue(0); issue(1);
    STAMP(1);
    asm volatile("s_waitcnt vmcnt(8)\n\ts_barrier" ::: "memory");
    STAMP(2);
    issue(2); issue(3);
    asm volatile("s_waitcnt vmcnt(16)\n\ts_barrier" ::: "memory");
    STAMP(3);
    issue(4);
    asm volatile("s_waitcnt vmcnt(16)\n\ts_barrier" ::: "memory");
    STAMP(4);
    issue(5);
    asm volatile("s_waitcnt vmcnt(16)\n\ts_barrier" ::: "memory");
    STAMP(5);
    issue(6);
    asm volatile("s_waitcnt vmcnt(16)\n\ts_barrier" ::: "memory");
    STAMP(6);
    asm volatile("s_waitcnt vmcnt(8)\n\ts_barrier" ::: "memory");
    STAMP(7);
    asm volatile("s_waitcnt vmcnt(0)\n\ts_barrier" ::: "memory");
    STAMP(8);
    if (g >= 7 && g <= 10) {
      const int m0 = 128 * (g - 7);
      for (int hd = (m0 + 27 - 27) / 28 + w; hd < 16 && 28 * hd + 27 < m0 + 128; hd += 4) {
        if (28 * hd + 27 >= m0) {
          const unsigned pv = (lane & 3) == 0 ? 0x3F803F80u : 0u;
          st16<AUX1>(ws + WS_KV + (size_t)(hd * 64 + 4 * t + (lane >> 4)) * 4096 + 2048 + (size_t)((lane >> 2) & 3) * 512 + (size_t)(28 + (lane & 3)) * 16, (u32x4){pv, pv, pv, pv});
        }
      }
    }
    STAMP(11); STAMP_OUT(0, w8);
    ENDPGM();
  }
  const int wr_ = w & 1, wc = w >> 1;
  f32x16 acc[2][2] = {};
  const int q = (lane & 15) >> 2, p = lane & 3, G = (lane >> 4) & 1;
  const unsigned xa = l0 + wr_ * 8192 + r * 128 + ((hh ^ ((r >> 1) & 7)) * 16);
  const unsigned wb = l0 + 16384 + wc * 8192 + (8 * hh + q) * 128 + (q >> 1) * 64;
  const int c0 = 128 * g + 64 * wc;
  if (g < 7) {
    k1_compute<1>(xa, wb + (4 * (p & 1) + 2 * G + (p >> 1)) * 8, acc K1_STAMP_PASS);
    STAMP(10);
    const bool isq = c0 < 448;
    const float sc = isq ? 0.1889822365046136f * 1.4426950408889634f : 1.f;
    const unsigned padv = isq ? 0u : 0x3F80u;
    char* base = ws + (isq ? WS_Q : WS_KV) + (size_t)r * 16;
    const unsigned hs = isq ? 131072u : 262144u, ts = isq ? 2048u : 4096u;
#pragma unroll
    for (int ct = 0; ct < 2; ++ct)
#pragma unroll
      for (int e = 0; e < 2; ++e) {
        const int g8 = (c0 - (isq ? 0 : 448)) / 8 + 4 * ct + 2 * hh + e;
        const int hp = (g8 * 37) >> 8, pos = g8 - 7 * hp;
        const unsigned off = (unsigned)(2 * hp + (pos > 3)) * hs + (unsigned)(pos > 3 ? pos - 3 : pos) * 512u;
#pragma unroll
        for (int rt = 0; rt < 2; ++rt) {
          const f32x16 v = acc[rt][ct] * sc;
          u32x4 d = e ? pack8<1>(v) : pack8<0>(v);
          char* dst = base + (size_t)(4 * t + 2 * wr_ + rt) * ts + off;
          if (pos == 3) { st16<AUX1>(dst + hs - 1536, (u32x4){padv, 0u, d[2], d[3]}); d[2] = padv; d[3] = 0u; }
          st16<AUX1>(dst, d);
        }
      }
  } else {
    k1_compute<0>(xa, wb + (4 * G + p) * 8, acc K1_STAMP_PASS);
    STAMP(10);
#pragma unroll
    for (int ct = 0; ct < 2; ++ct) {
      const int c = c0 + 32 * ct + r;
      char* dst; unsigned ts;
      if (c0 < 1344) { const int m = c - 896, hd = (m * 2341) >> 16, d = m - 28 * hd;
        dst = ws + WS_KV + (size_t)(hd * 64 + 4 * t + 2 * wr_) * 4096 + 2048 + (size_t)(d + 32 * hh) * 16; ts = 4096; }
      else { const int tile = 4 * t + 2 * wr_;
        dst = ws + WS_X + (size_t)(((c0 - 1344) >> 5) + ct) * 131072 + (size_t)tile * 2048 + (size_t)lane * 16; ts = 2048; }
#pragma unroll
      for (int rt = 0; rt < 2; ++rt) {
        st16<AUX1>(dst + rt * ts, pack8<0>(acc[rt][ct]));
        st16<AUX1>(dst + rt * ts + 1024, pack8<1>(acc[rt][ct]));
      }
    }
  }
  STAMP(11); STAMP_OUT(0, w8);
}

#define EX(x) x = __builtin_amdgcn_exp2f(x)
DI f32x16 lds_f16(unsigned a) { f32x16 v;
#pragma unroll
  for (int g = 0; g < 4; ++g) { const f32x4 c = *(const LDS_AS f32x4*)(size_t)(a + g * 1024);
#pragma unroll
    for (int e = 0; e < 4; ++e) v[4 * g + e] = c[e]; }
  return v; }
DI void fft_wave(int u, int lane, char* __restrict__ ws, unsigned l0) {
  const bool special = (u == 0) | (u == 224);
  const int cr = u == 224 ? 1 : 2 * u;
  const unsigned vo = lane * 16u;
  { const char* xs = ws + WS_X + (size_t)(cr >> 5) * 131072 + (size_t)(cr & 31) * 16; const char* ts = (const char*)&g_fft; const char* wsrc = ws + WS_WT + (size_t)u * 8192;
    const unsigned xo = (unsigned)(lane & 31) * 2048u + (unsigned)(lane >> 5) * 512u;
#pragma unroll
    for (int i = 0; i < 8; ++i) glds(l0 + i * 1024, xo, xs + (i >> 2) * 16 + ((i >> 1) & 1) * 65536 + (i & 1) * 1024);
#pragma unroll 1
    for (int i = 0; i < 11; ++i) glds4(l0 + 8192 + i * 4096, vo, ts + i * 4096);
    glds4(l0 + 52 * 1024, vo, wsrc); glds4(l0 + 56 * 1024, vo, wsrc + 4096);
#pragma unroll 1
    for (int i = 0; i < 3; ++i) glds4(l0 + (60 + 4 * i) * 1024, vo, ts + (44 + 4 * i) * 1024); }
  unsigned L = l0 + vo, L2 = L + 49152; PIN(L); PIN(L2);
#define FR(i) ((i) < 48 ? lds_b128(L + (i) * 1024) : lds_b128(L2 + ((i) - 48) * 1024))
  asm volatile("s_waitcnt vmcnt(44)" ::: "memory");
  u32x4 ar[4], ai[4];
  const unsigned zmask = special ? 0u : ~0u;
  const f32x16 twc = lds_f16(L + 20 * 1024), tws = lds_f16(L + 24 * 1024);
  {
    bf16x8 z[8], T[12];
#pragma unroll
    for (int i = 0; i < 8; ++i) z[i] = FR(i);
#pragma unroll
    for (int i = 0; i < 12; ++i) T[i] = FR(8 + i);
    SBAR();
#pragma unroll
    for (int i = 4; i < 8; ++i) z[i] = asbf(__builtin_bit_cast(u32x4, z[i]) & zmask);
    f32x16 re[2] = {}, im[2] = {};
#pragma unroll
    for (int rt = 0; rt < 2; ++rt)
#pragma unroll
      for (int s = 0; s < 2; ++s) {
        re[rt] = MFMA(z[rt * 2 + s], T[rt * 6 + s], re[rt]); im[rt] = MFMA(z[4 + rt * 2 + s], T[rt * 6 + s], im[rt]);
        re[rt] = MFMA(z[4 + rt * 2 + s], T[rt * 6 + 2 + s], re[rt]); im[rt] = MFMA(z[rt * 2 + s], T[rt * 6 + 4 + s], im[rt]);
      }
#pragma unroll
    for (int rt = 0; rt < 2; ++rt) {
      const f32x16 r2 = re[rt] * twc + im[rt] * tws, i2 = im[rt] * twc - re[rt] * tws;
      ar[rt * 2] = pack8<0>(r2); ar[rt * 2 + 1] = pack8<1>(r2); ai[rt * 2] = pack8<0>(i2); ai[rt * 2 + 1] = pack8<1>(i2);
    }
    SBAR();
  }
  asm volatile("s_waitcnt vmcnt(12)" ::: "memory");
  u32x4 ur[4], ui[4];
  bf16x8 T64[24];
#pragma unroll
  for (int i = 0; i < 24; ++i) T64[i] = FR(28 + i);
#pragma unroll
  for (int ft = 0; ft < 2; ++ft) {
    u32x4 wv[4];
#pragma unroll
    for (int g = 0; g < 4; ++g) wv[g] = __builtin_bit_cast(u32x4, FR(52 + ft * 4 + g));
    SBAR();
    f32x16 re = {}, im = {};
#pragma unroll
    for (int ks = 0; ks < 4; ++ks) {
      re = MFMA(T64[ft * 12 + ks], asbf(ar[ks]), re); im = MFMA(T64[ft * 12 + ks], asbf(ai[ks]), im);
      re = MFMA(T64[ft * 12 + 4 + ks], asbf(ai[ks]), re); im = MFMA(T64[ft * 12 + 8 + ks], asbf(ar[ks]), im);
    }
    f32x16 wr_, wi_;
#pragma unroll
    for (int g = 0; g < 4; ++g)
#pragma unroll
      for (int e = 0; e < 4; ++e) { wr_[4 * g + e] = __uint_as_float(wv[g][e] << 16); wi_[4 * g + e] = __uint_as_float(wv[g][e] & 0xffff0000u); }
    const f32x16 r2 = re * wr_ - im * wi_, i2 = re * wi_ + im * wr_;
    ur[ft * 2] = pack8<0>(r2); ur[ft * 2 + 1] = pack8<1>(r2); ui[ft * 2] = pack8<0>(i2); ui[ft * 2 + 1] = pack8<1>(i2);
    SBAR();
  }
  asm volatile("s_waitcnt vmcnt(0)" ::: "memory");
  const float osc = special ? 0.5f : 1.f;
#pragma unroll
  for (int ct = 0; ct < 2; ++ct) {
    bf16x8 T[6];
#pragma unroll
    for (int i = 0; i < 6; ++i) T[i] = FR(60 + ct * 6 + i);
    SBAR();
    f32x16 re = {}, im = {};
#pragma unroll
    for (int ks = 0; ks < 4; ++ks) {
      re = MFMA(asbf(ur[ks]), T64[ct * 12 + ks], re); im = MFMA(asbf(ui[ks]), T64[ct * 12 + ks], im);
      re = MFMA(asbf(ui[ks]), T64[ct * 12 + 8 + ks], re); im = MFMA(asbf(ur[ks]), T64[ct * 12 + 4 + ks], im);
    }
    const f32x16 r2 = re * twc - im * tws, i2 = im * twc + re * tws;
    const u32x4 br[2] = {pack8<0>(r2), pack8<1>(r2)}, bi[2] = {pack8<0>(i2), pack8<1>(i2)};
    f32x16 yr = {}, yi = {};
#pragma unroll
    for (int s = 0; s < 2; ++s) {
      yr = MFMA(T[s], asbf(br[s]), yr); yi = MFMA(T[s], asbf(bi[s]), yi);
      yr = MFMA(T[4 + s], asbf(bi[s]), yr); yi = MFMA(T[2 + s], asbf(br[s]), yi);
    }
    yr *= osc;
    u32x4* yd = (u32x4*)(ws + WS_Y + (size_t)cr * 4096 + (size_t)ct * 2048 + (size_t)(lane & 31) * 64 + (size_t)(lane >> 5) * 32);
    st16<AUX2>(yd, pack8<0>(yr)); st16<AUX2>(yd + 1, pack8<1>(yr));
    if (!special) { st16<AUX2>(yd + 256, pack8<0>(yi)); st16<AUX2>(yd + 257, pack8<1>(yi)); }
    SBAR();
  }
}

DI void attn_slow(int h, int tile, int lane, char* __restrict__ ws) {
  const int hh = lane >> 5;
  const u32x4* qp = (const u32x4*)(ws + WS_Q + (size_t)(h * 64 + tile) * 2048) + lane;
  const u32x4 q0 = qp[0], q1 = qp[64];
  const u32x4* kv = (const u32x4*)(ws + WS_KV + (size_t)h * 262144) + lane;
  float m = -INFINITY;
#pragma unroll 1
  for (int i = 0; i < 64; ++i) {
    f32x16 s = {}; s = MFMA(asbf(kv[i * 256]), asbf(q0), s); s = MFMA(asbf(kv[i * 256 + 64]), asbf(q1), s);
    m = fmaxf(m, max16(s));
  }
  m = halfmax(m);
  f32x16 o = {};
#pragma unroll 1
  for (int i = 0; i < 64; ++i) {
    f32x16 s = {}; s = MFMA(asbf(kv[i * 256]), asbf(q0), s); s = MFMA(asbf(kv[i * 256 + 64]), asbf(q1), s);
#pragma unroll
    for (int j = 0; j < 16; ++j) s[j] = __builtin_amdgcn_exp2f(s[j] - m);
    o = MFMA(asbf(kv[i * 256 + 128]), asbf(pack8<0>(s)), o); o = MFMA(asbf(kv[i * 256 + 192]), asbf(pack8<1>(s)), o);
  }
  auto rr = __builtin_amdgcn_permlane32_swap(__float_as_uint(o[12]), __float_as_uint(o[12]), false, false);
  const float rl = 1.f / __uint_as_float(rr[1]);
  o *= rl;
  u32x4 o0 = pack8<0>(o), o1 = pack8<1>(o);
  if (hh == 1) { o1[2] = 0; o1[3] = 0; }
  u32x4* od = (u32x4*)(ws + WS_O + (size_t)(tile * 16 + h) * 2048) + lane;
  od[0] = o0; od[64] = o1;
}

#define FFT_PRIO 3
#define FFT_SLEEP 8
#define N0 13
#define N1 17
#define N2 17
__global__ __launch_bounds__(320, 1) void k2_kernel(const char* __restrict__ wo, char* __restrict__ ws) {
  __shared__ __attribute__((aligned(1024))) char lds[73728 + 49152 + 256];
  const int tid = threadIdx.x, lane = tid & 63, hh = lane >> 5;
  const int w = __builtin_amdgcn_readfirstlane(tid >> 6);
  const int b = blockIdx.x, xcd = b & 7, bi = b >> 3;
  STAMP_DECL; STAMP(0);
  if (w == 4) {
    __builtin_amdgcn_s_sleep(FFT_SLEEP);
    __builtin_amdgcn_s_setprio(FFT_PRIO);
    const int u = 32 * xcd + bi;
    { const unsigned o0 = (bi * 98 + lane) * 128u, o1 = (bi * 98 + 34 + lane) * 128u, lz = (unsigned)(size_t)lds + 73728 + 49152;
      touch(lz, o0, wo); touch(lz, o1, wo); touch(lz, o0, &g_gtab); touch(lz, o1, &g_gtab); }
    if (u < 225) fft_wave(u, lane, ws, (unsigned)(size_t)lds);
    STAMP(11); STAMP_OUT(1, w);
    ENDPGM();
  }
  const int h = 2 * xcd + (bi & 1), t = bi >> 1;
  const int t0 = w == 0 ? 0 : w == 1 ? N0 : w == 2 ? N0 + N1 : N0 + N1 + N2;
  const int t1 = w == 0 ? N0 : w == 1 ? N0 + N1 : w == 2 ? N0 + N1 + N2 : 64;
  const int n = t1 - t0;
  int tc = t0 + ((t * n) >> 4);
  auto nxt = [&](int v) __attribute__((always_inline)) { return v + 1 == t1 ? t0 : v + 1; };
  const auto kvr = __builtin_amdgcn_make_buffer_rsrc((void*)(ws + WS_KV + (size_t)h * 262144), 0, 262144, 0x00020000);
  const unsigned vo = lane * 16u;
#define KV(tile, idx) __builtin_bit_cast(u32x4, __builtin_amdgcn_raw_buffer_load_b128(kvr, vo + (idx) * 1024, (tile) * 4096, 0))
  const u32x4* qp = (const u32x4*)(ws + WS_Q + (size_t)(h * 64 + 4 * t) * 2048) + lane;
  const u32x4 kr0 = KV(4 * t, 0), kr1 = KV(4 * t, 1);
  u32x4 qa0 = qp[0], qa1 = qp[64], qb0 = qp[128], qb1 = qp[192], qc0 = qp[256], qc1 = qp[320], qd0 = qp[384], qd1 = qp[448];
  SBAR();
  u32x4 kA0 = KV(tc, 0), kA1 = KV(tc, 1); SBAR();
  u32x4 vA0 = KV(tc, 2); SBAR();
  u32x4 vA1 = KV(tc, 3); SBAR();
  int tv = nxt(tc);
  u32x4 kB0 = KV(tv, 0); SBAR(); u32x4 kB1 = KV(tv, 1); SBAR();
  u32x4 vB0 = KV(tv, 2), vB1 = {0, 0, 0, 0}; SBAR();
  tc = tv;
  f32x16 oa = {}, ob = {}, oc = {}, od = {}, sa, sb;
  const f32x16 zero = {};
  u32x4 p0, p1 = {0, 0, 0, 0};
  PIN(oa); PIN(ob); PIN(oc); PIN(od); PIN(p1); PIN(vB1); SBAR();
  {
    const bool selA = (h & 1) && hh == 0, selB = !(h & 1) && hh == 1;
    f32x16 s0 = MFMA(asbf(kr0), asbf(qa0), zero); s0 = MFMA(asbf(kr1), asbf(qa1), s0);
    f32x16 s1 = MFMA(asbf(kr0), asbf(qb0), zero); s1 = MFMA(asbf(kr1), asbf(qb1), s1);
    f32x16 s2 = MFMA(asbf(kr0), asbf(qc0), zero); s2 = MFMA(asbf(kr1), asbf(qc1), s2);
    f32x16 s3 = MFMA(asbf(kr0), asbf(qd0), zero); s3 = MFMA(asbf(kr1), asbf(qd1), s3);
#define REF(S, Q0, Q1) do { const unsigned nm = pk(-max16(S), 0.f); Q0[0] = selA ? nm : Q0[0]; Q1[2] = selB ? nm : Q1[2]; } while (0)
    REF(s0, qa0, qa1);
    sa = MFMA(asbf(kA0), asbf(qa0), zero); sa = MFMA(asbf(kA1), asbf(qa1), sa);
    REF(s1, qb0, qb1); REF(s2, qc0, qc1); REF(s3, qd0, qd1);
  }
  sb = zero;
  PIN(sa); STAMP(1); CSTAMP(8);
#define SLOT(S, N, KN0, KN1, QN0, QN1, OP, VP, OC, VC, LD1, LD2)                                                              \
  do {                                                                                                                        \
    SBAR();                                                                                                                   \
    EX(S[0]); EX(S[1]); p1[2] = pk(N[12], N[13]); EX(S[2]); EX(S[3]); p1[3] = pk(N[14], N[15]); PIN(S); PIN(p1); SBAR();      \
    N = MFMA(asbf(KN0), asbf(QN0), zero); SBAR();                                                                             \
    EX(S[4]); EX(S[5]); p0[0] = pk(S[0], S[1]); EX(S[6]); EX(S[7]); p0[1] = pk(S[2], S[3]); PIN(S); PIN(p0); SBAR();          \
    N = MFMA(asbf(KN1), asbf(QN1), N); SBAR();                                                                                \
    LD1;                                                                                                                      \
    EX(S[8]); EX(S[9]); p0[2] = pk(S[4], S[5]); EX(S[10]); EX(S[11]); p0[3] = pk(S[6], S[7]); PIN(S); PIN(p0); SBAR();        \
    OP = MFMA(asbf(VP), asbf(p1), OP); SBAR();                                                                                \
    EX(S[12]); EX(S[13]); p1[0] = pk(S[8], S[9]); EX(S[14]); EX(S[15]); p1[1] = pk(S[10], S[11]); PIN(S); PIN(p1); SBAR();    \
    OC = MFMA(asbf(VC), asbf(p0), OC); SBAR();                                                                                \
    LD2;                                                                                                                      \
  } while (0)
  for (int i = 1; i < n; i += 2) {
    const int tn = nxt(tc);
    SLOT(sa, sb, kA0, kA1, qb0, qb1, od, vB1, oa, vA0, (void)0, vB1 = KV(tv, 3));
    SLOT(sb, sa, kA0, kA1, qc0, qc1, oa, vA1, ob, vA0, (void)0, (void)0);
    SLOT(sa, sb, kA0, kA1, qd0, qd1, ob, vA1, oc, vA0, (kA0 = KV(tn, 0), kA1 = KV(tn, 1)), (void)0);
    SLOT(sb, sa, kB0, kB1, qa0, qa1, oc, vA1, od, vA0, (void)0, vA0 = KV(tn, 2));
    tc = nxt(tn);
    SLOT(sa, sb, kB0, kB1, qb0, qb1, od, vA1, oa, vB0, (void)0, vA1 = KV(tn, 3));
    SLOT(sb, sa, kB0, kB1, qc0, qc1, oa, vB1, ob, vB0, (void)0, (void)0);
    SLOT(sa, sb, kB0, kB1, qd0, qd1, ob, vB1, oc, vB0, (kB0 = KV(tc, 0), kB1 = KV(tc, 1)), (void)0);
    SLOT(sb, sa, kA0, kA1, qa0, qa1, oc, vB1, od, vB0, (void)0, vB0 = KV(tc, 2));
    tv = tc;
  }
  SLOT(sa, sb, kA0, kA1, qb0, qb1, od, vB1, oa, vA0, (void)0, (void)0);
  SLOT(sb, sa, kA0, kA1, qc0, qc1, oa, vA1, ob, vA0, (void)0, (void)0);
  SLOT(sa, sb, kA0, kA1, qd0, qd1, ob, vA1, oc, vA0, (void)0, (void)0);
  SLOT(sb, sa, kA0, kA1, qa0, qa1, oc, vA1, od, vA0, (void)0, (void)0);
  p1[2] = pk(sb[12], sb[13]); p1[3] = pk(sb[14], sb[15]);
  od = MFMA(asbf(vA1), asbf(p1), od);
  PIN(oa); PIN(od); STAMP(2); CSTAMP(9);
  f32x4* cw = (f32x4*)(lds + 73728) + lane;
#define PUT(O, DST) do { if (w != DST) { const int slot = DST * 3 + (w > DST ? w - 1 : w); _Pragma("unroll") for (int i = 0; i < 4; ++i) \
      cw[slot * 256 + i * 64] = (f32x4){O[4 * i], O[4 * i + 1], O[4 * i + 2], O[4 * i + 3]}; } } while (0)
  PUT(oa, 0); PUT(ob, 1); PUT(oc, 2); PUT(od, 3);
  f32x16 o = w == 0 ? oa : w == 1 ? ob : w == 2 ? oc : od;
  STAMP(3);
  __syncthreads();
  STAMP(4);
#pragma unroll
  for (int j = 0; j < 3; ++j)
#pragma unroll
    for (int i = 0; i < 4; ++i) { const f32x4 v = cw[(w * 3 + j) * 256 + i * 64]; o[4 * i] += v[0]; o[4 * i + 1] += v[1]; o[4 * i + 2] += v[2]; o[4 * i + 3] += v[3]; }
  auto rr = __builtin_amdgcn_permlane32_swap(__float_as_uint(o[12]), __float_as_uint(o[12]), false, false);
  const float l = __uint_as_float(rr[1]);
  float amax = 0.f, nanchk = 0.f;
#pragma unroll
  for (int i = 0; i < 16; i += 2) { amax = fmaxf(fmaxf(amax, fabsf(o[i])), fabsf(o[i + 1])); nanchk += o[i] + o[i + 1]; }
  const bool ok = (l >= 1.0e-12f) && (l <= 1.0e12f) && (amax <= 1.0e37f) && (nanchk == nanchk);
  if (__builtin_expect(!__all(ok), 0)) {
    attn_slow(h, 4 * t + w, lane, ws);
    return;
  }
  o *= 1.f / l;
  u32x4 o0 = pack8<0>(o), o1 = pack8<1>(o);
  if (hh == 1) { o1[2] = 0; o1[3] = 0; }
  u32x4* odst = (u32x4*)(ws + WS_O + (size_t)((4 * t + w) * 16 + h) * 2048) + lane;
  st16<AUX2>(odst, o0); st16<AUX2>(odst + 64, o1);
  STAMP(11); STAMP_OUT(1, w);
}

#define GLD(dst, voff, sb, imm) asm volatile("global_load_dwordx4 %0, %1, %2 offset:" #imm : "=v"(dst) : "v"(voff), "s"(sb) : "memory")
__global__ __launch_bounds__(512, 2) void k3_kernel(const char* __restrict__ wo, const char* __restrict__ ws, char* __restrict__ out) {
  __shared__ __attribute__((aligned(1024))) char lds[2 * 57344 + 16384];
  const int tid = threadIdx.x, lane = tid & 63, r = lane & 31, hh = lane >> 5;
  const int w8 = __builtin_amdgcn_readfirstlane(tid >> 6), w = w8 & 3;
  const int b = blockIdx.x, rb = 4 * (b & 7) + (b >> 3) / 7, cb = (b >> 3) % 7;
  const unsigned l0 = (unsigned)(size_t)lds;
  STAMP_DECL; STAMP(0);
  if (w8 < 4) {
    const unsigned sw = (unsigned)(((lane & 7) ^ (4 * ((lane >> 4) & 1))) * 16);
    { const unsigned so = (unsigned)(lane >> 3) * 896u + sw; const char* sb = wo + cb * 128 + (size_t)w * 112 * 896;
#pragma unroll
      for (int j = 0; j < 14; ++j) glds(l0 + 57344 + w * 14336 + j * 1024, so, sb + (size_t)j * 8 * 896); }
    asm volatile("s_barrier" ::: "memory");
    const unsigned so = (unsigned)(lane >> 3) * 4096u + sw;
    const char* sb = ws + WS_Y + rb * 128 + (size_t)w * 8 * 4096;
    auto issue = [&](int st) __attribute__((always_inline)) {
#pragma unroll
      for (int j = 0; j < 4; ++j) { const int pc = (j == 3 && w >= 2) ? 2 : j; glds(l0 + st * 14336 + (pc * 4 + w) * 1024, so, sb + (size_t)(st * 112 + pc * 32) * 4096); } };
    issue(0); issue(1);
    asm volatile("s_waitcnt vmcnt(4)\n\ts_barrier" ::: "memory");
    issue(2);
    asm volatile("s_waitcnt vmcnt(4)\n\ts_barrier" ::: "memory");
    issue(3);
    asm volatile("s_waitcnt vmcnt(4)\n\ts_barrier" ::: "memory");
    asm volatile("s_waitcnt vmcnt(0)\n\ts_barrier" ::: "memory");
    STAMP(11); STAMP_OUT(2, w8);
    ENDPGM();
  }
  const bool isb = w >= 2; const int dt = w & 1;
  const unsigned vo = lane * 16u;
  u32x4 f[4][8];
  const int frt = dt, fct = isb ? 1 - dt : dt;
  float g;
  f32x16 acc0 = {}, acc1 = {};
  const int q = (lane & 15) >> 2, p = lane & 3, G = (lane >> 4) & 1;
#define K3_WAIT(ST, N) asm volatile("s_waitcnt vmcnt(" #N ")\n\ts_barrier" : "+v"(f[ST][0]), "+v"(f[ST][1]), "+v"(f[ST][2]), "+v"(f[ST][3]), "+v"(f[ST][4]), \
               "+v"(f[ST][5]), "+v"(f[ST][6]), "+v"(f[ST][7])::"memory")
  if (!isb) {
    const char* db = (const char*)&g_gtab + (size_t)(2 * cb + dt) * 28672;
#pragma unroll
    for (int st = 0; st < 4; ++st) { const char* d0 = db + st * 7168; const char* d1 = d0 + 4096;
      GLD(f[st][0], vo, d0, 0); GLD(f[st][1], vo, d0, 1024); GLD(f[st][2], vo, d0, 2048); GLD(f[st][3], vo, d0, 3072);
      GLD(f[st][4], vo, d1, 0); GLD(f[st][5], vo, d1, 1024); GLD(f[st][6], vo, d1, 2048); f[st][7] = (u32x4){0, 0, 0, 0}; }
    asm volatile("global_load_dword %0, %1, %2" : "=v"(g) : "v"(r * 4u), "s"(ws + WS_G + (size_t)(rb * 64 + frt * 32) * 4) : "memory");
    asm volatile("s_barrier" ::: "memory");
    STAMP(1);
    unsigned y0 = l0 + (8 * hh + q) * 128 + ((q >> 1) * 64) + G * 32 + p * 8, y1 = y0 ^ 64; PIN(y0); PIN(y1);
    bf16x8 a0, a1, b0, b1;
#define K3_RA(D0, D1, KS) do { D0 = lds_tr2(y0 + (KS) * 2048, y0 + (KS) * 2048 + 512); D1 = lds_tr2(y1 + (KS) * 2048, y1 + (KS) * 2048 + 512); SBAR(); } while (0)
#define K3_MA(D0, D1, F) do { acc0 = MFMA(asbf(F), D0, acc0); acc1 = MFMA(asbf(F), D1, acc1); SBAR(); } while (0)
#define K3_STA(ST) K3_WAIT(ST, 0); STAMP(2 + ST);                                                                          \
    K3_RA(a0, a1, ST * 7); K3_RA(b0, b1, ST * 7 + 1); K3_MA(a0, a1, f[ST][0]); K3_RA(a0, a1, ST * 7 + 2); K3_MA(b0, b1, f[ST][1]); \
    K3_RA(b0, b1, ST * 7 + 3); K3_MA(a0, a1, f[ST][2]); K3_RA(a0, a1, ST * 7 + 4); K3_MA(b0, b1, f[ST][3]);               \
    K3_RA(b0, b1, ST * 7 + 5); K3_MA(a0, a1, f[ST][4]); K3_RA(a0, a1, ST * 7 + 6); K3_MA(b0, b1, f[ST][5]); K3_MA(a0, a1, f[ST][6]);
    K3_STA(0) K3_STA(1) K3_STA(2) K3_STA(3)
  } else {
    { const unsigned ln = cb * 74 + dt * 37 + (lane < 37 ? lane : 0);
      touch(l0 + 114688 + w * 256, (ln < 512 ? ln : 0) * 128u, ws + WS_O + (size_t)rb * 65536);
      touch(l0 + 114688 + w * 256, (unsigned)(cb * 64 + dt * 32 + r) * 4096u, ws + WS_Y + rb * 128); }
    asm volatile("global_load_dword %0, %1, %2" : "=v"(g) : "v"(r * 4u), "s"(ws + WS_G + (size_t)(rb * 64 + frt * 32) * 4) : "memory");
    asm volatile("s_barrier" ::: "memory");
    STAMP(1);
    const char* db = ws + WS_O + (size_t)(2 * rb + dt) * 32768;
#define K3_LOAD(I) do { const char* d0 = db + I * 8192; const char* d1 = d0 + 4096;                                              \
    GLD(f[I][0], vo, d0, 0); GLD(f[I][1], vo, d0, 1024); GLD(f[I][2], vo, d0, 2048); GLD(f[I][3], vo, d0, 3072);                  \
    GLD(f[I][4], vo, d1, 0); GLD(f[I][5], vo, d1, 1024); GLD(f[I][6], vo, d1, 2048); GLD(f[I][7], vo, d1, 3072); } while (0)
    K3_LOAD(0); K3_LOAD(1);
    unsigned w0 = l0 + 57344 + (4 * hh + q) * 128 + ((q >> 1) * 64) + (4 * (p & 1) + 2 * G + (p >> 1)) * 8, w1 = w0 ^ 64;
    unsigned w2 = w0 - hh * 512, w3 = w1 - hh * 512;
    PIN(w0); PIN(w1); PIN(w2); PIN(w3);
    bf16x8 a0, a1, b0, b1;
#define K3_OFF(J) (((J) >> 1) * 3584 + ((J) & 1) * 2048)
#define K3_RB(D0, D1, J) do { D0 = lds_tr2(w0 + K3_OFF(J), (((J) & 1) ? w2 : w0) + K3_OFF(J) + 1024); D1 = lds_tr2(w1 + K3_OFF(J), (((J) & 1) ? w3 : w1) + K3_OFF(J) + 1024); SBAR(); } while (0)
#define K3_MB(D0, D1, F) do { acc0 = MFMA(D0, asbf(F), acc0); acc1 = MFMA(D1, asbf(F), acc1); SBAR(); } while (0)
#define K3_STB(ST, N, NEXT) K3_WAIT(ST, N); STAMP(2 + ST);                                                                  \
    K3_RB(a0, a1, ST * 8); K3_RB(b0, b1, ST * 8 + 1); K3_MB(a0, a1, f[ST][0]); K3_RB(a0, a1, ST * 8 + 2); K3_MB(b0, b1, f[ST][1]); \
    K3_RB(b0, b1, ST * 8 + 3); K3_MB(a0, a1, f[ST][2]); K3_RB(a0, a1, ST * 8 + 4); K3_MB(b0, b1, f[ST][3]);               \
    K3_RB(b0, b1, ST * 8 + 5); K3_MB(a0, a1, f[ST][4]); K3_RB(a0, a1, ST * 8 + 6); K3_MB(b0, b1, f[ST][5]);               \
    K3_RB(b0, b1, ST * 8 + 7); K3_MB(a0, a1, f[ST][6]); NEXT; K3_MB(b0, b1, f[ST][7]);
    K3_STB(0, 8, K3_LOAD(2)) K3_STB(1, 8, K3_LOAD(3)) K3_STB(2, 8, (void)0) K3_STB(3, 0, (void)0)
  }
  asm volatile("" : "+v"(g));
  PIN(acc0); PIN(acc1); STAMP(6);
  const bool keep1 = isb ? (dt == 0) : (dt == 1);
  const f32x16 mine = keep1 ? acc1 : acc0, send = keep1 ? acc0 : acc1;
  f32x4* ex = (f32x4*)(lds + 114688) + lane;
  const int to = isb ? dt : 3 - dt;
#pragma unroll
  for (int i = 0; i < 4; ++i) ex[to * 256 + i * 64] = (f32x4){send[4 * i], send[4 * i + 1], send[4 * i + 2], send[4 * i + 3]};
  const float ga = g * (2.0f / (2048.f * 448.f)), g1 = rbf(1.f - g);
  const f32x16 part = mine * (isb ? g1 : ga);
  const float go = isb ? ga : g1;
  __syncthreads();
  f32x16 res;
#pragma unroll
  for (int i = 0; i < 4; ++i) { const f32x4 v = ex[w * 256 + i * 64];
#pragma unroll
    for (int e = 0; e < 4; ++e) res[4 * i + e] = fmaf(v[e], go, part[4 * i + e]); }
  const int srow = 64 * r + 2 * rb + frt;
  u32x4* od = (u32x4*)(out + (size_t)srow * 896 + (size_t)(cb * 64 + fct * 32 + 16 * hh) * 2);
  st16<AUX3>(od, pack8<0>(res)); st16<AUX3>(od + 1, pack8<1>(res));
  STAMP(11); STAMP_OUT(2, w8);
}

extern "C" void kernel_launch(void* const* d_in, const int* in_sizes, int n_in, void* d_out, int out_size, void* d_ws, size_t ws_size, hipStream_t stream) {
  char* ws = (char*)d_ws;
  k1_kernel<<<256, 512, 0, stream>>>((const char*)d_in[0], (const char*)d_in[4], (const char*)d_in[6], (const char*)d_in[1], (const char*)d_in[2], ws);
  k2_kernel<<<256, 320, 0, stream>>>((const char*)d_in[5], ws);
  k3_kernel<<<224, 512, 0, stream>>>((const char*)d_in[5], ws, (char*)d_out);
}
```

```cpp
#include <hip/hip_runtime.h>
#include <stdint.h>

typedef short bf16x8 __attribute__((ext_vector_type(8)));
typedef short s16x4 __attribute__((ext_vector_type(4)));
typedef float f32x16 __attribute__((ext_vector_type(16)));
typedef float f32x4 __attribute__((ext_vector_type(4)));
typedef float f32x2 __attribute__((ext_vector_type(2)));
typedef unsigned u32x4 __attribute__((ext_vector_type(4)));
typedef __bf16 bf16x2_t __attribute__((ext_vector_type(2)));
#define K1_MAP 1
#define DI __device__ __forceinline__
#define LDS_AS __attribute__((address_space(3)))

namespace tab {
constexpr double PI = 3.14159265358979323846264338327950288;
constexpr double tsin(double x) { double x2 = x * x, t = x, s = x; for (int i = 1; i <= 10; ++i) { t *= -x2 / ((2 * i) * (2 * i + 1)); s += t; } return s; }
constexpr double tcos(double x) { double x2 = x * x, t = 1, s = 1; for (int i = 1; i <= 10; ++i) { t *= -x2 / ((2 * i - 1) * (2 * i)); s += t; } return s; }
constexpr double cosr(int m, int N) {
  m %= N; if (m < 0) m += N; if (2 * m > N) m = N - m;
  bool neg = false; if (4 * m > N) { m = N / 2 - m; neg = true; }
  double r = (8 * m > N) ? tsin(2 * PI * (N / 4 - m) / N) : tcos(2 * PI * m / N); return neg ? -r : r; }
constexpr double sinr(int m, int N) { return cosr(m - N / 4, N); }
constexpr uint32_t f2bf(double d) { float f = (float)d; uint32_t u = __builtin_bit_cast(uint32_t, f); u += 0x7fffu + ((u >> 16) & 1u); return u >> 16; }
constexpr int crow(int r, int hh) { return (r & 3) + 8 * (r >> 2) + 4 * hh; }
constexpr int krow(int s, int hh, int j) { return 16 * s + 8 * (j >> 2) + 4 * hh + (j & 3); }
constexpr int sigma(int r) { return 16 * ((r >> 2) & 1) + 4 * (r >> 3) + (r & 3); }
struct Base448 { uint16_t c[448], ns[448]; };
constexpr Base448 mk448() { Base448 b{}; for (int i = 0; i < 448; ++i) { b.c[i] = f2bf(cosr(i, 448)); b.ns[i] = f2bf(-sinr(i, 448)); } return b; }
constexpr Base448 B448 = mk448();
constexpr uint32_t Fval(int j, int c) { return c == 1 ? B448.c[(j * 224) % 448] : ((c & 1) ? B448.ns[(j * (c >> 1)) % 448] : B448.c[(j * (c >> 1)) % 448]); }
struct Tile { uint32_t v[28 * 64 * 4]; };
constexpr Tile mkF(int jt) { Tile t{}; for (int j = 0; j < 32; ++j) for (int c = 0; c < 224; ++c) t.v[j * 224 + c] = Fval(jt * 32 + j, 2 * c) | (Fval(jt * 32 + j, 2 * c + 1) << 16); return t; }
constexpr Tile mkG(int jt) { Tile t{}; for (int ks = 0; ks < 28; ++ks) for (int l = 0; l < 64; ++l) { int j = jt * 32 + sigma(l & 31), c0 = ks * 16 + 8 * (l >> 5);
  for (int e = 0; e < 4; ++e) t.v[(ks * 64 + l) * 4 + e] = Fval(j, c0 + 2 * e) | (Fval(j, c0 + 2 * e + 1) << 16); } return t; }
template <int T> constexpr Tile FT = mkF(T);
template <int T> constexpr Tile GT = mkG(T);
struct Tab14 { Tile t[14]; };
struct Fft { uint32_t t1[12][256]; float tw[8][256]; uint32_t t64[24][256]; uint32_t t4[12][256]; };
struct Base64 { uint16_t v[3][64]; };
constexpr Base64 mk64() { Base64 b{}; for (int i = 0; i < 64; ++i) { b.v[0][i] = f2bf(cosr(i, 64)); b.v[1][i] = f2bf(sinr(i, 64)); b.v[2][i] = f2bf(-sinr(i, 64)); } return b; }
constexpr Base64 B64 = mk64();
constexpr uint32_t cs64(int m, int which) { return B64.v[which][m & 63]; }
constexpr Fft mkFft() { Fft f{};
  for (int l = 0; l < 64; ++l) { const int r = l & 31, hh = l >> 5;
    for (int e = 0; e < 4; ++e) {
      for (int t = 0; t < 2; ++t) for (int w = 0; w < 3; ++w) for (int s = 0; s < 2; ++s) {
        const int k0 = krow(s, hh, 2 * e), k1 = krow(s, hh, 2 * e + 1), n1 = sigma(r);
        f.t1[(t * 3 + w) * 2 + s][l * 4 + e] = cs64((2 * k0 + t) * r, w) | (cs64((2 * k1 + t) * r, w) << 16);
        f.t4[(t * 3 + w) * 2 + s][l * 4 + e] = cs64((2 * n1 + t) * k0, w) | (cs64((2 * n1 + t) * k1, w) << 16); }
      for (int t = 0; t < 2; ++t) for (int w = 0; w < 3; ++w) for (int ks = 0; ks < 4; ++ks) {
        const int a = 32 * t + r, b0 = 32 * (ks >> 1) + krow(ks & 1, hh, 2 * e), b1 = 32 * (ks >> 1) + krow(ks & 1, hh, 2 * e + 1);
        f.t64[(t * 3 + w) * 4 + ks][l * 4 + e] = cs64(a * b0, w) | (cs64(a * b1, w) << 16); }
      for (int g = 0; g < 4; ++g) { const int m = crow(4 * g + e, hh) * r; f.tw[g][l * 4 + e] = (float)cosr(m, 2048); f.tw[4 + g][l * 4 + e] = (float)sinr(m, 2048); }
    } }
  return f; }
constexpr Fft FFT = mkFft();
}
#define TAB14(X) {{tab::X<0>, tab::X<1>, tab::X<2>, tab::X<3>, tab::X<4>, tab::X<5>, tab::X<6>, tab::X<7>, tab::X<8>, tab::X<9>, tab::X<10>, tab::X<11>, tab::X<12>, tab::X<13>}}
__device__ __attribute__((aligned(256))) const tab::Tab14 g_ftab = TAB14(FT);
__device__ __attribute__((aligned(256))) const tab::Tab14 g_gtab = TAB14(GT);
__device__ __attribute__((aligned(256))) const tab::Fft g_fft = tab::FFT;

constexpr size_t WS_Q = 0;
constexpr size_t WS_KV = WS_Q + (2u << 20);
constexpr size_t WS_X = WS_KV + (4u << 20);
constexpr size_t WS_WT = WS_X + (2u << 20);
constexpr size_t WS_G = WS_WT + (2u << 20);
constexpr size_t WS_FLG = WS_G + 65536;
constexpr size_t WS_O = WS_G + (1u << 20);
constexpr size_t WS_Y = WS_O + (2u << 20);

#define STAMP_DECL
#define STAMP(i)
#define STAMP_OUT(K, W)
#define CSTAMP(i)
#define MFMA(a, b, c) __builtin_amdgcn_mfma_f32_32x32x16_bf16((a), (b), (c), 0, 0, 0)
#define SBAR() __builtin_amdgcn_sched_barrier(0)
#define PIN(x) asm volatile("" : "+v"(x))
DI unsigned pk(float lo, float hi) { f32x2 v = {lo, hi}; return __builtin_bit_cast(unsigned, __builtin_convertvector(v, bf16x2_t)); }
DI bf16x8 asbf(u32x4 v) { return __builtin_bit_cast(bf16x8, v); }
template <int S> DI u32x4 pack8(const f32x16& a) { return (u32x4){pk(a[8 * S], a[8 * S + 1]), pk(a[8 * S + 2], a[8 * S + 3]), pk(a[8 * S + 4], a[8 * S + 5]), pk(a[8 * S + 6], a[8 * S + 7])}; }
DI float rbf(float v) { return __uint_as_float(pk(v, v) << 16); }
DI void glds(unsigned lds, unsigned voff, const void* sbase) {
  asm volatile("s_mov_b32 m0, %0\n\ts_nop 0\n\tglobal_load_lds_dwordx4 %1, %2" ::"s"(lds), "v"(voff), "s"(sbase) : "memory"); }
DI void glds4(unsigned lds, unsigned voff, const void* sbase) {
  asm volatile("s_mov_b32 m0, %0\n\ts_nop 0\n\tglobal_load_lds_dwordx4 %1, %2\n\tglobal_load_lds_dwordx4 %1, %2 offset:1024\n\t"
               "global_load_lds_dwordx4 %1, %2 offset:2048\n\tglobal_load_lds_dwordx4 %1, %2 offset:3072" ::"s"(lds), "v"(voff), "s"(sbase) : "memory"); }
DI void glds_dev(unsigned lds, unsigned voff, const void* sbase) {
  asm volatile("s_mov_b32 m0, %0\n\ts_nop 0\n\tglobal_load_lds_dwordx4 %1, %2 sc1" ::"s"(lds), "v"(voff), "s"(sbase) : "memory"); }
DI void glds4_dev(unsigned lds, unsigned voff, const void* sbase) {
  asm volatile("s_mov_b32 m0, %0\n\ts_nop 0\n\tglobal_load_lds_dwordx4 %1, %2 sc1\n\tglobal_load_lds_dwordx4 %1, %2 offset:1024 sc1\n\t"
               "global_load_lds_dwordx4 %1, %2 offset:2048 sc1\n\tglobal_load_lds_dwordx4 %1, %2 offset:3072 sc1" ::"s"(lds), "v"(voff), "s"(sbase) : "memory"); }
typedef __attribute__((address_space(1))) unsigned gu32;
DI void set_flag(char* p) { __hip_atomic_store((gu32*)p, 1u, __ATOMIC_RELAXED, __HIP_MEMORY_SCOPE_AGENT); }
template <int SLEEP> DI void wait_flags(const char* p) {
#pragma unroll 1
  for (int spin = 0; spin < (1 << 20); ++spin) {
    const unsigned v = __hip_atomic_load((gu32*)p, __ATOMIC_RELAXED, __HIP_MEMORY_SCOPE_AGENT);
    if (__all(v != 0u)) break;
    __builtin_amdgcn_s_sleep(SLEEP);
  }
  asm volatile("" ::: "memory");
}
DI void touch(unsigned lds, unsigned voff, const void* sbase) {
  asm volatile("s_mov_b32 m0, %0\n\ts_nop 0\n\tglobal_load_lds_dword %1, %2" ::"s"(lds), "v"(voff), "s"(sbase) : "memory"); }
#define AUX1 16
#define AUX2 16
#define AUX3 0
typedef unsigned v4u_t __attribute__((ext_vector_type(4)));
template <int AUX> DI void st16(void* p, u32x4 v) {
  if (AUX == 0) *(u32x4*)p = v;
  else if (AUX == 2) asm volatile("global_store_dwordx4 %0, %1, off nt\n\ts_nop 1" ::"v"(p), "v"(v) : "memory");
  else if (AUX == 16) asm volatile("global_store_dwordx4 %0, %1, off sc1\n\ts_nop 1" ::"v"(p), "v"(v) : "memory");
  else asm volatile("global_store_dwordx4 %0, %1, off sc0 sc1\n\ts_nop 1" ::"v"(p), "v"(v) : "memory");
}
#define ENDPGM() __builtin_amdgcn_endpgm()
typedef short v4i16_t __attribute__((ext_vector_type(4)));
DI bf16x8 lds_b128(unsigned a) { return *(const LDS_AS bf16x8*)(size_t)a; }
DI bf16x8 lds_tr2(unsigned a0, unsigned a1) {
  v4i16_t lo = __builtin_amdgcn_ds_read_tr16_b64_v4i16((LDS_AS v4i16_t*)(size_t)a0), hi = __builtin_amdgcn_ds_read_tr16_b64_v4i16((LDS_AS v4i16_t*)(size_t)a1);
  return __builtin_shufflevector(lo, hi, 0, 1, 2, 3, 4, 5, 6, 7); }
DI float halfmax(float m) { auto r = __builtin_amdgcn_permlane32_swap(__float_as_uint(m), __float_as_uint(m), false, false); return fmaxf(__uint_as_float(r[0]), __uint_as_float(r[1])); }
DI float max16(const f32x16& s) { float m = fmaxf(s[0], s[1]);
#pragma unroll
  for (int i = 2; i < 16; i += 2) m = fmaxf(fmaxf(m, s[i]), s[i + 1]);
  return m; }

constexpr int K1_SLOT = 32768;
struct K1Frag { bf16x8 x0, x1, w0, w1; };
struct K1Addr { unsigned x[4], w0, w1; };
DI void k1_read(K1Frag& f, const K1Addr& A, unsigned o, int ks) {
  f.x0 = lds_b128(A.x[ks] + o); f.w0 = lds_tr2(A.w0 + o + ks * 2048, A.w0 + o + ks * 2048 + 512);
  f.x1 = lds_b128(A.x[ks] + o + 4096); f.w1 = lds_tr2(A.w1 + o + ks * 2048, A.w1 + o + ks * 2048 + 512);
}
template <int TR> DI void k1_mma(const K1Frag& f, f32x16 (&acc)[2][2]) {
  if (TR) { acc[0][0] = MFMA(f.w0, f.x0, acc[0][0]); acc[1][0] = MFMA(f.w0, f.x1, acc[1][0]); acc[0][1] = MFMA(f.w1, f.x0, acc[0][1]); acc[1][1] = MFMA(f.w1, f.x1, acc[1][1]); }
  else    { acc[0][0] = MFMA(f.x0, f.w0, acc[0][0]); acc[1][0] = MFMA(f.x1, f.w0, acc[1][0]); acc[0][1] = MFMA(f.x0, f.w1, acc[0][1]); acc[1][1] = MFMA(f.x1, f.w1, acc[1][1]); }
}
#define K1_STAMP_ARG
#define K1_STAMP_PASS
template <int TR> DI void k1_compute(unsigned xa, unsigned wa, f32x16 (&acc)[2][2] K1_STAMP_ARG) {
  K1Addr A;
#pragma unroll
  for (int ks = 0; ks < 4; ++ks) A.x[ks] = xa ^ (ks * 32);
  A.w0 = wa; A.w1 = wa ^ 64;
  K1Frag fa, fb = {};
#define K1_RD(F, KS, M0, M1, M2, M3, G) do {                                                                                     \
    F.x0 = lds_b128(A.x[KS]); SBAR(); M0(G); SBAR();                                                                              \
    F.w0 = lds_tr2(A.w0 + KS * 2048, A.w0 + KS * 2048 + 512); if (KS == 3) { A.w0 += d; PIN(A.w0); } SBAR(); M1(G); SBAR();       \
    F.x1 = lds_b128(A.x[KS] + 4096); A.x[KS] += d; PIN(A.x[KS]); SBAR(); M2(G); SBAR();                                           \
    F.w1 = lds_tr2(A.w1 + KS * 2048, A.w1 + KS * 2048 + 512); if (KS == 3) { A.w1 += d; PIN(A.w1); } SBAR(); M3(G); SBAR(); } while (0)
#define K1_M0(G) acc[0][0] = TR ? MFMA(G.w0, G.x0, acc[0][0]) : MFMA(G.x0, G.w0, acc[0][0])
#define K1_M1(G) acc[1][0] = TR ? MFMA(G.w0, G.x1, acc[1][0]) : MFMA(G.x1, G.w0, acc[1][0])
#define K1_M2(G) acc[0][1] = TR ? MFMA(G.w1, G.x0, acc[0][1]) : MFMA(G.x0, G.w1, acc[0][1])
#define K1_M3(G) acc[1][1] = TR ? MFMA(G.w1, G.x1, acc[1][1]) : MFMA(G.x1, G.w1, acc[1][1])
  __builtin_amdgcn_s_waitcnt(0xC07F);
#pragma unroll 1
  for (int i = 0; i < 7; ++i) {
    asm volatile("s_barrier" ::: "memory");
    const unsigned d = i == 4 ? (unsigned)(-4 * K1_SLOT) : (unsigned)K1_SLOT;
    K1_RD(fa, 0, K1_M0, K1_M1, K1_M2, K1_M3, fb);
    K1_RD(fb, 1, K1_M0, K1_M1, K1_M2, K1_M3, fa);
    K1_RD(fa, 2, K1_M0, K1_M1, K1_M2, K1_M3, fb);
    K1_RD(fb, 3, K1_M0, K1_M1, K1_M2, K1_M3, fa);
  }
  k1_mma<TR>(fb, acc);
  PIN(acc[0][0]); PIN(acc[1][1]); STAMP(2); CSTAMP(9);
}

__global__ __launch_bounds__(512, 2) void k1_kernel(const char* __restrict__ x, const char* __restrict__ wqkv, const char* __restrict__ gw,
                                                     const char* __restrict__ wr, const char* __restrict__ wi, const char* __restrict__ wo, char* __restrict__ ws) {
  __shared__ __attribute__((aligned(1024))) char lds[5 * K1_SLOT];
  const int tid = threadIdx.x, lane = tid & 63, r = lane & 31, hh = lane >> 5;
  const int w8 = __builtin_amdgcn_readfirstlane(tid >> 6), w = w8 & 3;
  const int b = blockIdx.x, xcd = b & 7, bi = b >> 3;
  const unsigned l0 = (unsigned)(size_t)lds;
  STAMP_DECL; STAMP(0);
  if (bi >= 28) {
    const int id = xcd * 4 + (bi - 28), fq = id >> 1, f1h = id & 1;
    { const char* src = (w8 < 4 ? wr : wi) + (size_t)(128 * fq + 32 * w + 16 * f1h) * 450;
      const unsigned lb = l0 + w8 * 8192;
      glds4(lb, lane * 16u, src);
      glds(lb + 4096, lane * 16u, src + 4096); glds(lb + 5120, lane * 16u, src + 5120); glds(lb + 6144, lane * 16u, src + 6144);
      glds(lb + 7168, (lane < 2 ? lane : 0) * 16u, src + 7168); }
    const int gp = 64 * id + 8 * w8 + (lane >> 3);
    u32x4 gx[7], gv[7];
    { const unsigned xo = (unsigned)(64 * (gp & 31) + (gp >> 5)) * 896u + (lane & 7) * 16u, go = (lane & 7) * 16u;
#define GLDG(i) asm volatile("global_load_dwordx4 %0, %2, %4 offset:" #i "*128\n\tglobal_load_dwordx4 %1, %3, %5 offset:" #i "*128" : "=&v"(gx[i]), "=&v"(gv[i]) : "v"(xo), "v"(go), "s"(x), "s"(gw) : "memory")
      GLDG(0); GLDG(1); GLDG(2); GLDG(3); GLDG(4); GLDG(5); GLDG(6); }
    { const unsigned wi_ = (bi - 28) * 8 + w8, lz = l0 + 65536 + w8 * 256;
      const int hx = 2 * xcd + (wi_ >> 4), cbx = hx >= 7 ? (hx >= 14 ? 0 : hx - 7) : hx; const unsigned ln = (wi_ & 15) * 28 + (lane < 28 ? lane : 0);
      touch(lz, (wi_ * 14 + (lane < 14 ? lane : 0)) * 128u, &g_fft);
      touch(lz, ln * 896u, wo + cbx * 128); touch(lz, ln * 128u, (const char*)&g_gtab + cbx * 57344); }
    if (id == 0 && tid < 320) st16<16>(ws + WS_FLG + tid * 16, (u32x4){0u, 0u, 0u, 0u});
    asm volatile("s_waitcnt vmcnt(17)\n\ts_barrier" ::: "memory");
    { const int f1l = tid & 15, k0 = tid >> 4;
      const LDS_AS unsigned short* src = (const LDS_AS unsigned short*)(size_t)(l0 + f1l * 450 + k0 * 2);
      char* dst = ws + WS_WT + (size_t)k0 * 8192 + (size_t)(fq >> 1) * 1024 + (size_t)(32 * (fq & 1) + 16 * f1h + f1l) * 16;
      u32x4 v[8];
#pragma unroll
      for (int it = 0; it < 8; ++it)
#pragma unroll
        for (int e = 0; e < 4; ++e) v[it][e] = (unsigned)src[e * 4096 + it * 32] | ((unsigned)src[16384 + e * 4096 + it * 32] << 16);
      asm volatile("s_waitcnt vmcnt(3)" : "+v"(gx[0]), "+v"(gx[1]), "+v"(gx[2]), "+v"(gx[3]), "+v"(gx[4]), "+v"(gx[5]), "+v"(gx[6]),
                   "+v"(gv[0]), "+v"(gv[1]), "+v"(gv[2]), "+v"(gv[3]), "+v"(gv[4]), "+v"(gv[5]), "+v"(gv[6])::"memory");
#pragma unroll
      for (int it = 0; it < 8; ++it) if (it < 7 || k0 == 0) st16<AUX1>(dst + (size_t)it * 32 * 8192, v[it]);
    }
    { float z = 0.f;
#pragma unroll
      for (int i = 0; i < 7; ++i)
#pragma unroll
        for (int e = 0; e < 4; ++e) { const unsigned xa_ = gx[i][e], ga_ = gv[i][e]; z = __builtin_amdgcn_fdot2_f32_bf16(__builtin_bit_cast(bf16x2_t, xa_), __builtin_bit_cast(bf16x2_t, ga_), z, false); }
      z += __shfl_xor(z, 1); z += __shfl_xor(z, 2); z += __shfl_xor(z, 4);
      if ((lane & 7) == 0) *(float*)(ws + WS_G + (size_t)gp * 4) = rbf(1.f / (1.f + __expf(-rbf(z)))); }
    STAMP(9); STAMP(11); STAMP_OUT(0, w8);
    ENDPGM();
  }
  const int t = 4 * (xcd >> 1) + (bi & 3), g = 7 * (xcd & 1) + (bi >> 2);
  if (w8 < 4) {
    const int rot = (g + 2 * t) % 7;
    const unsigned xo_e = (unsigned)(lane >> 3) * (64u * 896u) + (unsigned)(((lane & 7) ^ (lane >> 4)) * 16);
    const unsigned xo_o = xo_e ^ 64u;
    const char* xb = x + (size_t)(4 * t + w) * 896;
    const int c0 = 128 * g + 64 * (w >> 1);
    const unsigned rs = c0 < 1344 ? 2688u : 896u;
    const char* cb = (c0 < 1344 ? wqkv + c0 * 2 : (const char*)&g_ftab + (c0 - 1344) * 2) + (size_t)(32 * (w & 1)) * rs;
    const unsigned co = (unsigned)(lane >> 3) * rs + (unsigned)(((lane & 7) ^ (4 * ((lane >> 4) & 1))) * 16);
    auto issue = [&](int i) __attribute__((always_inline)) {
      int c = i + rot; c = c >= 7 ? c - 7 : c;
      const unsigned lb = l0 + (i % 5) * K1_SLOT + w * 4096;
      const char* xc = xb + c * 128; const char* cc = cb + (size_t)(c * 64) * rs;
#pragma unroll
      for (int j = 0; j < 4; ++j) {
        glds(lb + j * 1024, (j & 1) ? xo_o : xo_e, xc + j * (8 * 64 * 896));
        glds(lb + 16384 + j * 1024, co, cc + j * 8 * rs);
      }
    };
    issue(0); issue(1);
    STAMP(1);
    asm volatile("s_waitcnt vmcnt(8)\n\ts_barrier" ::: "memory");
    STAMP(2);
    issue(2); issue(3);
    asm volatile("s_waitcnt vmcnt(16)\n\ts_barrier" ::: "memory");
    STAMP(3);
    issue(4);
    asm volatile("s_waitcnt vmcnt(16)\n\ts_barrier" ::: "memory");
    STAMP(4);
    issue(5);
    asm volatile("s_waitcnt vmcnt(16)\n\ts_barrier" ::: "memory");
    STAMP(5);
    issue(6);
    asm volatile("s_waitcnt vmcnt(16)\n\ts_barrier" ::: "memory");
    STAMP(6);
    asm volatile("s_waitcnt vmcnt(8)\n\ts_barrier" ::: "memory");
    STAMP(7);
    asm volatile("s_waitcnt vmcnt(0)\n\ts_barrier" ::: "memory");
    STAMP(8);
    if (g >= 7 && g <= 10) {
      const int m0 = 128 * (g - 7);
      for (int hd = (m0 + 27 - 27) / 28 + w; hd < 16 && 28 * hd + 27 < m0 + 128; hd += 4) {
        if (28 * hd + 27 >= m0) {
          const unsigned pv = (lane & 3) == 0 ? 0x3F803F80u : 0u;
          st16<AUX1>(ws + WS_KV + (size_t)(hd * 64 + 4 * t + (lane >> 4)) * 4096 + 2048 + (size_t)((lane >> 2) & 3) * 512 + (size_t)(28 + (lane & 3)) * 16, (u32x4){pv, pv, pv, pv});
        }
      }
    }
    STAMP(11); STAMP_OUT(0, w8);
    ENDPGM();
  }
  const int wr_ = w & 1, wc = w >> 1;
  f32x16 acc[2][2] = {};
  const int q = (lane & 15) >> 2, p = lane & 3, G = (lane >> 4) & 1;
  const unsigned xa = l0 + wr_ * 8192 + r * 128 + ((hh ^ ((r >> 1) & 7)) * 16);
  const unsigned wb = l0 + 16384 + wc * 8192 + (8 * hh + q) * 128 + (q >> 1) * 64;
  const int c0 = 128 * g + 64 * wc;
  if (g < 7) {
    k1_compute<1>(xa, wb + (4 * (p & 1) + 2 * G + (p >> 1)) * 8, acc K1_STAMP_PASS);
    STAMP(10);
    const bool isq = c0 < 448;
    const float sc = isq ? 0.1889822365046136f * 1.4426950408889634f : 1.f;
    const unsigned padv = isq ? 0u : 0x3F80u;
    char* base = ws + (isq ? WS_Q : WS_KV) + (size_t)r * 16;
    const unsigned hs = isq ? 131072u : 262144u, ts = isq ? 2048u : 4096u;
#pragma unroll
    for (int ct = 0; ct < 2; ++ct)
#pragma unroll
      for (int e = 0; e < 2; ++e) {
        const int g8 = (c0 - (isq ? 0 : 448)) / 8 + 4 * ct + 2 * hh + e;
        const int hp = (g8 * 37) >> 8, pos = g8 - 7 * hp;
        const unsigned off = (unsigned)(2 * hp + (pos > 3)) * hs + (unsigned)(pos > 3 ? pos - 3 : pos) * 512u;
#pragma unroll
        for (int rt = 0; rt < 2; ++rt) {
          const f32x16 v = acc[rt][ct] * sc;
          u32x4 d = e ? pack8<1>(v) : pack8<0>(v);
          char* dst = base + (size_t)(4 * t + 2 * wr_ + rt) * ts + off;
          if (pos == 3) { st16<AUX1>(dst + hs - 1536, (u32x4){padv, 0u, d[2], d[3]}); d[2] = padv; d[3] = 0u; }
          st16<AUX1>(dst, d);
        }
      }
  } else {
    k1_compute<0>(xa, wb + (4 * G + p) * 8, acc K1_STAMP_PASS);
    STAMP(10);
#pragma unroll
    for (int ct = 0; ct < 2; ++ct) {
      const int c = c0 + 32 * ct + r;
      char* dst; unsigned ts;
      if (c0 < 1344) { const int m = c - 896, hd = (m * 2341) >> 16, d = m - 28 * hd;
        dst = ws + WS_KV + (size_t)(hd * 64 + 4 * t + 2 * wr_) * 4096 + 2048 + (size_t)(d + 32 * hh) * 16; ts = 4096; }
      else { const int tile = 4 * t + 2 * wr_;
        dst = ws + WS_X + (size_t)(((c0 - 1344) >> 5) + ct) * 131072 + (size_t)tile * 2048 + (size_t)lane * 16; ts = 2048; }
#pragma unroll
      for (int rt = 0; rt < 2; ++rt) {
        st16<AUX1>(dst + rt * ts, pack8<0>(acc[rt][ct]));
        st16<AUX1>(dst + rt * ts + 1024, pack8<1>(acc[rt][ct]));
      }
    }
  }
  STAMP(11); STAMP_OUT(0, w8);
}

#define EX(x) x = __builtin_amdgcn_exp2f(x)
DI f32x16 lds_f16(unsigned a) { f32x16 v;
#pragma unroll
  for (int g = 0; g < 4; ++g) { const f32x4 c = *(const LDS_AS f32x4*)(size_t)(a + g * 1024);
#pragma unroll
    for (int e = 0; e < 4; ++e) v[4 * g + e] = c[e]; }
  return v; }
#define FSTAMP(i)
#define FFT_GAP 0
DI f32x16 FMFMA(bf16x8 a, bf16x8 b, f32x16 c) {
  f32x16 d = MFMA(a, b, c);
  if (FFT_GAP == 1) { SBAR(); __builtin_amdgcn_s_sleep(1); SBAR(); }
  if (FFT_GAP == 2) { SBAR(); asm volatile("s_nop 15\n\ts_nop 15"); SBAR(); }
  if (FFT_GAP == 3) { SBAR(); asm volatile("s_nop 15\n\ts_nop 15\n\ts_nop 15"); SBAR(); }
  return d; }
DI void fft_wave(int u, int lane, char* __restrict__ ws, unsigned l0 K1_STAMP_ARG) {
  const bool special = (u == 0) | (u == 224);
  const int cr = u == 224 ? 1 : 2 * u;
  const unsigned vo = lane * 16u;
  { const char* xs = ws + WS_X + (size_t)(cr >> 5) * 131072 + (size_t)(cr & 31) * 16; const char* ts = (const char*)&g_fft; const char* wsrc = ws + WS_WT + (size_t)u * 8192;
    const unsigned xo = (unsigned)(lane & 31) * 2048u + (unsigned)(lane >> 5) * 512u;
#pragma unroll
    for (int i = 0; i < 8; ++i) glds(l0 + i * 1024, xo, xs + (i >> 2) * 16 + ((i >> 1) & 1) * 65536 + (i & 1) * 1024);
#pragma unroll 1
    for (int i = 0; i < 11; ++i) glds4(l0 + 8192 + i * 4096, vo, ts + i * 4096);
    glds4(l0 + 52 * 1024, vo, wsrc); glds4(l0 + 56 * 1024, vo, wsrc + 4096);
#pragma unroll 1
    for (int i = 0; i < 3; ++i) glds4(l0 + (60 + 4 * i) * 1024, vo, ts + (44 + 4 * i) * 1024); }
  unsigned L = l0 + vo, L2 = L + 49152; PIN(L); PIN(L2);
#define FR(i) ((i) < 48 ? lds_b128(L + (i) * 1024) : lds_b128(L2 + ((i) - 48) * 1024))
  FSTAMP(1);
  asm volatile("s_waitcnt vmcnt(44)" ::: "memory");
  FSTAMP(2);
  u32x4 ar[4], ai[4];
  const unsigned zmask = special ? 0u : ~0u;
  const f32x16 twc = lds_f16(L + 20 * 1024), tws = lds_f16(L + 24 * 1024);
  {
    bf16x8 z[8], T[12];
#pragma unroll
    for (int i = 0; i < 8; ++i) z[i] = FR(i);
#pragma unroll
    for (int i = 0; i < 12; ++i) T[i] = FR(8 + i);
    SBAR();
#pragma unroll
    for (int i = 4; i < 8; ++i) z[i] = asbf(__builtin_bit_cast(u32x4, z[i]) & zmask);
    f32x16 re[2] = {}, im[2] = {};
#pragma unroll
    for (int rt = 0; rt < 2; ++rt)
#pragma unroll
      for (int s = 0; s < 2; ++s) {
        re[rt] = FMFMA(z[rt * 2 + s], T[rt * 6 + s], re[rt]); im[rt] = FMFMA(z[4 + rt * 2 + s], T[rt * 6 + s], im[rt]);
        re[rt] = FMFMA(z[4 + rt * 2 + s], T[rt * 6 + 2 + s], re[rt]); im[rt] = FMFMA(z[rt * 2 + s], T[rt * 6 + 4 + s], im[rt]);
      }
#pragma unroll
    for (int rt = 0; rt < 2; ++rt) {
      const f32x16 r2 = re[rt] * twc + im[rt] * tws, i2 = im[rt] * twc - re[rt] * tws;
      ar[rt * 2] = pack8<0>(r2); ar[rt * 2 + 1] = pack8<1>(r2); ai[rt * 2] = pack8<0>(i2); ai[rt * 2 + 1] = pack8<1>(i2);
    }
    SBAR();
  }
  FSTAMP(3);
  asm volatile("s_waitcnt vmcnt(12)" ::: "memory");
  FSTAMP(4);
  u32x4 ur[4], ui[4];
  bf16x8 T64[24];
#pragma unroll
  for (int i = 0; i < 24; ++i) T64[i] = FR(28 + i);
#pragma unroll
  for (int ft = 0; ft < 2; ++ft) {
    u32x4 wv[4];
#pragma unroll
    for (int g = 0; g < 4; ++g) wv[g] = __builtin_bit_cast(u32x4, FR(52 + ft * 4 + g));
    SBAR();
    f32x16 re = {}, im = {};
#pragma unroll
    for (int ks = 0; ks < 4; ++ks) {
      re = FMFMA(T64[ft * 12 + ks], asbf(ar[ks]), re); im = FMFMA(T64[ft * 12 + ks], asbf(ai[ks]), im);
      re = FMFMA(T64[ft * 12 + 4 + ks], asbf(ai[ks]), re); im = FMFMA(T64[ft * 12 + 8 + ks], asbf(ar[ks]), im);
    }
    f32x16 wr_, wi_;
#pragma unroll
    for (int g = 0; g < 4; ++g)
#pragma unroll
      for (int e = 0; e < 4; ++e) { wr_[4 * g + e] = __uint_as_float(wv[g][e] << 16); wi_[4 * g + e] = __uint_as_float(wv[g][e] & 0xffff0000u); }
    const f32x16 r2 = re * wr_ - im * wi_, i2 = re * wi_ + im * wr_;
    ur[ft * 2] = pack8<0>(r2); ur[ft * 2 + 1] = pack8<1>(r2); ui[ft * 2] = pack8<0>(i2); ui[ft * 2 + 1] = pack8<1>(i2);
    SBAR();
  }
  FSTAMP(5);
  asm volatile("s_waitcnt vmcnt(0)" ::: "memory");
  FSTAMP(6);
  const float osc = special ? 0.5f : 1.f;
#pragma unroll
  for (int ct = 0; ct < 2; ++ct) {
    bf16x8 T[6];
#pragma unroll
    for (int i = 0; i < 6; ++i) T[i] = FR(60 + ct * 6 + i);
    SBAR();
    f32x16 re = {}, im = {};
#pragma unroll
    for (int ks = 0; ks < 4; ++ks) {
      re = FMFMA(asbf(ur[ks]), T64[ct * 12 + ks], re); im = FMFMA(asbf(ui[ks]), T64[ct * 12 + ks], im);
      re = FMFMA(asbf(ui[ks]), T64[ct * 12 + 8 + ks], re); im = FMFMA(asbf(ur[ks]), T64[ct * 12 + 4 + ks], im);
    }
    const f32x16 r2 = re * twc - im * tws, i2 = im * twc + re * tws;
    const u32x4 br[2] = {pack8<0>(r2), pack8<1>(r2)}, bi[2] = {pack8<0>(i2), pack8<1>(i2)};
    f32x16 yr = {}, yi = {};
#pragma unroll
    for (int s = 0; s < 2; ++s) {
      yr = FMFMA(T[s], asbf(br[s]), yr); yi = FMFMA(T[s], asbf(bi[s]), yi);
      yr = FMFMA(T[4 + s], asbf(bi[s]), yr); yi = FMFMA(T[2 + s], asbf(br[s]), yi);
    }
    yr *= osc;
    u32x4* yd = (u32x4*)(ws + WS_Y + (size_t)cr * 4096 + (size_t)ct * 2048 + (size_t)(lane & 31) * 64 + (size_t)(lane >> 5) * 32);
    st16<AUX2>(yd, pack8<0>(yr)); st16<AUX2>(yd + 1, pack8<1>(yr));
    if (!special) { st16<AUX2>(yd + 256, pack8<0>(yi)); st16<AUX2>(yd + 257, pack8<1>(yi)); }
    SBAR();
  }
}

DI f32x16 attn_slow(int h, int tile, int lane, const char* __restrict__ ws) {
  const u32x4* qp = (const u32x4*)(ws + WS_Q + (size_t)(h * 64 + tile) * 2048) + lane;
  const u32x4 q0 = qp[0], q1 = qp[64];
  const u32x4* kv = (const u32x4*)(ws + WS_KV + (size_t)h * 262144) + lane;
  float m = -INFINITY;
#pragma unroll 1
  for (int i = 0; i < 64; ++i) {
    f32x16 s = {}; s = MFMA(asbf(kv[i * 256]), asbf(q0), s); s = MFMA(asbf(kv[i * 256 + 64]), asbf(q1), s);
    m = fmaxf(m, max16(s));
  }
  m = halfmax(m);
  f32x16 o = {};
#pragma unroll 1
  for (int i = 0; i < 64; ++i) {
    f32x16 s = {}; s = MFMA(asbf(kv[i * 256]), asbf(q0), s); s = MFMA(asbf(kv[i * 256 + 64]), asbf(q1), s);
#pragma unroll
    for (int j = 0; j < 16; ++j) s[j] = __builtin_amdgcn_exp2f(s[j] - m);
    o = MFMA(asbf(kv[i * 256 + 128]), asbf(pack8<0>(s)), o); o = MFMA(asbf(kv[i * 256 + 192]), asbf(pack8<1>(s)), o);
  }
  return o;
}

#define FFT_PRIO 3
#define FFT_SLEEP 8
#define N0 13
#define N1 17
#define N2 17
#define GLD(dst, voff, sb, imm) asm volatile("global_load_dwordx4 %0, %1, %2 offset:" #imm : "=v"(dst) : "v"(voff), "s"(sb) : "memory")
#define BAR() asm volatile("s_barrier" ::: "memory")
__global__ __launch_bounds__(512, 2) void k2_kernel(const char* __restrict__ wo, char* __restrict__ ws, char* __restrict__ out) {
  __shared__ __attribute__((aligned(1024))) char lds[163840];
  const int tid = threadIdx.x, lane = tid & 63, hh = lane >> 5;
  const int w8 = __builtin_amdgcn_readfirstlane(tid >> 6), w = w8 & 3;
  const int b = blockIdx.x, xcd = b & 7, bi = b >> 3;
  const int h = 2 * xcd + (bi & 1), t = bi >> 1;
  const unsigned l0 = (unsigned)(size_t)lds;
  const unsigned vo = lane * 16u;
  STAMP_DECL; STAMP(0);
  if (w8 >= 4) {
    const int rb = 2 * t + (h >= 7), cb = h >= 7 ? h - 7 : h, rt = w >> 1, ct = w & 1, r = lane & 31;
    const unsigned sw = (unsigned)(((lane & 7) ^ (4 * ((lane >> 4) & 1))) * 16);
    if (w == 0) {
      __builtin_amdgcn_s_sleep(FFT_SLEEP);
      __builtin_amdgcn_s_setprio(FFT_PRIO);
      const int u = 32 * xcd + bi;
      if (u < 225) fft_wave(u, lane, ws, l0 + 90112 K1_STAMP_PASS);
      asm volatile("s_waitcnt vmcnt(0)" ::: "memory");
      if (lane == 0) set_flag(ws + WS_FLG + u * 4);
      __builtin_amdgcn_s_setprio(0);
    } else {
      __builtin_amdgcn_s_sleep(100);
      if (h < 14) { const unsigned so = (unsigned)(lane >> 3) * 896u + sw; const char* sb = wo + cb * 128;
#pragma unroll 1
        for (int j = w - 1; j < 56; j += 3) { glds(l0 + j * 1024, so, sb + (size_t)j * 8 * 896); __builtin_amdgcn_s_sleep(2); } }
    }
    STAMP(1);
    if (h >= 14) { STAMP(11); STAMP_OUT(1, w8); ENDPGM(); }
    u32x4 f[32];
    float g;
    { const char* db = (const char*)&g_gtab + (size_t)(2 * cb + ct) * 28672;
#define GLD4(I) do { const char* d0 = db + (I) * 1024; GLD(f[I], vo, d0, 0); GLD(f[I + 1], vo, d0, 1024); GLD(f[I + 2], vo, d0, 2048); GLD(f[I + 3], vo, d0, 3072); __builtin_amdgcn_s_sleep(4); } while (0)
      GLD4(0); GLD4(4); GLD4(8); GLD4(12); GLD4(16); GLD4(20); GLD4(24);
      asm volatile("global_load_dword %0, %1, %2" : "=v"(g) : "v"(r * 4u), "s"(ws + WS_G + (size_t)(rb * 64 + rt * 32) * 4) : "memory"); }
#define F8(I) "+v"(f[I]), "+v"(f[I + 1]), "+v"(f[I + 2]), "+v"(f[I + 3]), "+v"(f[I + 4]), "+v"(f[I + 5]), "+v"(f[I + 6])
    asm volatile("s_waitcnt vmcnt(0)" : F8(0), F8(7), F8(14), F8(21), "+v"(g)::"memory");
    STAMP(2);
    if (w == 0) {
#pragma unroll
      for (int i = 0; i < 4; ++i) wait_flags<16>(ws + WS_FLG + (i * 64 + lane) * 4); }
    STAMP(3);
    BAR();
    { const unsigned so = (unsigned)(lane >> 3) * 4096u + sw; const char* sb = ws + WS_Y + rb * 128 + (size_t)w * 14 * 8 * 4096;
#pragma unroll
      for (int j = 0; j < 14; ++j) glds_dev(l0 + 57344 + (w * 14 + j) * 1024, so, sb + (size_t)j * 8 * 4096); }
    BAR();
    asm volatile("s_waitcnt vmcnt(0)" ::: "memory");
    STAMP(4);
    BAR();
    STAMP(5);
    const int q = (lane & 15) >> 2, p = lane & 3, G = (lane >> 4) & 1;
    f32x16 acca = {}, accb = {};
    { unsigned ya = l0 + 57344 + (8 * hh + q) * 128 + (((q >> 1) ^ rt) * 64) + G * 32 + p * 8; PIN(ya);
      bf16x8 y[4];
#pragma unroll
      for (int i = 0; i < 4; ++i) y[i] = lds_tr2(ya + i * 2048, ya + i * 2048 + 512);
#pragma unroll
      for (int i = 0; i < 28; ++i) { SBAR(); acca = MFMA(asbf(f[i]), y[i & 3], acca); SBAR(); if (i + 4 < 28) y[i & 3] = lds_tr2(ya + (i + 4) * 2048, ya + (i + 4) * 2048 + 512); } }
    SBAR();
    STAMP(6);
    if (w == 0) wait_flags<1>(ws + WS_FLG + 1024 + rb * 128 + r * 4);
    BAR();
    STAMP(8);
    { const char* ob_ = ws + WS_O + (size_t)(2 * rb + rt) * 32768 + ct * 8192; const unsigned lb = l0 + 57344 + rt * 16384 + ct * 8192;
      glds4_dev(lb, vo, ob_); glds4_dev(lb + 4096, vo, ob_ + 4096);
      glds4_dev(lb + 32768, vo, ob_ + 16384); glds4_dev(lb + 32768 + 4096, vo, ob_ + 16384 + 4096); }
    { unsigned w0 = l0 + (4 * hh + q) * 128 + (((q >> 1) ^ ct) * 64) + (4 * (p & 1) + 2 * G + (p >> 1)) * 8, w2 = w0 - hh * 512; PIN(w0); PIN(w2);
#pragma unroll
      for (int j = 0; j < 32; ++j) { const unsigned o_ = (j >> 1) * 3584 + (j & 1) * 2048; f[j] = __builtin_bit_cast(u32x4, lds_tr2(w0 + o_, ((j & 1) ? w2 : w0) + o_ + 1024)); } }
    acca *= g * (2.0f / (2048.f * 448.f));
    const float g1 = rbf(1.f - g);
    unsigned oa_ = l0 + 57344 + rt * 16384 + vo; PIN(oa_);
    __builtin_amdgcn_s_waitcnt(0xC07F);
#pragma unroll
    for (int hf = 0; hf < 2; ++hf) {
      if (hf == 0) asm volatile("s_waitcnt vmcnt(8)\n\ts_barrier" ::: "memory"); else asm volatile("s_waitcnt vmcnt(0)\n\ts_barrier" ::: "memory");
      STAMP(7 + 3 * hf);
      bf16x8 y[4];
#pragma unroll
      for (int i = 0; i < 4; ++i) y[i] = lds_b128(oa_ + hf * 32768 + i * 1024);
#pragma unroll
      for (int i = 0; i < 16; ++i) { SBAR(); accb = MFMA(asbf(f[hf * 16 + i]), y[i & 3], accb); SBAR(); if (i + 4 < 16) y[i & 3] = lds_b128(oa_ + hf * 32768 + (i + 4) * 1024); }
    }
    const f32x16 res = accb * g1 + acca;
    u32x4* od_ = (u32x4*)(out + (size_t)(64 * r + 2 * rb + rt) * 896 + (size_t)(cb * 64 + ct * 32 + 16 * hh) * 2);
    st16<AUX3>(od_, pack8<0>(res)); st16<AUX3>(od_ + 1, pack8<1>(res));
    STAMP(11); STAMP_OUT(1, w8);
    ENDPGM();
  }
  const int t0 = w == 0 ? 0 : w == 1 ? N0 : w == 2 ? N0 + N1 : N0 + N1 + N2;
  const int t1 = w == 0 ? N0 : w == 1 ? N0 + N1 : w == 2 ? N0 + N1 + N2 : 64;
  const int n = t1 - t0;
  int tc = t0 + ((t * n) >> 4);
  auto nxt = [&](int v) __attribute__((always_inline)) { return v + 1 == t1 ? t0 : v + 1; };
  const auto kvr = __builtin_amdgcn_make_buffer_rsrc((void*)(ws + WS_KV + (size_t)h * 262144), 0, 262144, 0x00020000);
#define KV(tile, idx) __builtin_bit_cast(u32x4, __builtin_amdgcn_raw_buffer_load_b128(kvr, vo + (idx) * 1024, (tile) * 4096, 0))
  const u32x4* qp = (const u32x4*)(ws + WS_Q + (size_t)(h * 64 + 4 * t) * 2048) + lane;
  const u32x4 kr0 = KV(4 * t, 0), kr1 = KV(4 * t, 1);
  const int ia = ((w + 1) & 3) * 128, ib = ((w + 2) & 3) * 128, ic = ((w + 3) & 3) * 128, id_ = w * 128;
  u32x4 qa0 = qp[ia], qa1 = qp[ia + 64], qb0 = qp[ib], qb1 = qp[ib + 64], qc0 = qp[ic], qc1 = qp[ic + 64], qd0 = qp[id_], qd1 = qp[id_ + 64];
  SBAR();
  u32x4 kA0 = KV(tc, 0), kA1 = KV(tc, 1); SBAR();
  u32x4 vA0 = KV(tc, 2); SBAR();
  u32x4 vA1 = KV(tc, 3); SBAR();
  int tv = nxt(tc);
  u32x4 kB0 = KV(tv, 0); SBAR(); u32x4 kB1 = KV(tv, 1); SBAR();
  u32x4 vB0 = KV(tv, 2), vB1 = {0, 0, 0, 0}; SBAR();
  tc = tv;
  f32x16 oa = {}, ob = {}, oc = {}, od = {}, sa, sb;
  const f32x16 zero = {};
  u32x4 p0, p1 = {0, 0, 0, 0};
  PIN(oa); PIN(ob); PIN(oc); PIN(od); PIN(p1); PIN(vB1); SBAR();
  {
    const bool selA = (h & 1) && hh == 0, selB = !(h & 1) && hh == 1;
    f32x16 s0 = MFMA(asbf(kr0), asbf(qa0), zero); s0 = MFMA(asbf(kr1), asbf(qa1), s0);
    f32x16 s1 = MFMA(asbf(kr0), asbf(qb0), zero); s1 = MFMA(asbf(kr1), asbf(qb1), s1);
    f32x16 s2 = MFMA(asbf(kr0), asbf(qc0), zero); s2 = MFMA(asbf(kr1), asbf(qc1), s2);
    f32x16 s3 = MFMA(asbf(kr0), asbf(qd0), zero); s3 = MFMA(asbf(kr1), asbf(qd1), s3);
#define REF(S, Q0, Q1) do { const unsigned nm = pk(-max16(S), 0.f); Q0[0] = selA ? nm : Q0[0]; Q1[2] = selB ? nm : Q1[2]; } while (0)
    REF(s0, qa0, qa1);
    sa = MFMA(asbf(kA0), asbf(qa0), zero); sa = MFMA(asbf(kA1), asbf(qa1), sa);
    REF(s1, qb0, qb1); REF(s2, qc0, qc1); REF(s3, qd0, qd1);
  }
  sb = zero;
  PIN(sa); STAMP(1); CSTAMP(8);
#define SLOT(S, N, KN0, KN1, QN0, QN1, OP, VP, OC, VC, LD1, LD2)                                                              \
  do {                                                                                                                        \
    SBAR();                                                                                                                   \
    EX(S[0]); EX(S[1]); p1[2] = pk(N[12], N[13]); EX(S[2]); EX(S[3]); p1[3] = pk(N[14], N[15]); PIN(S); PIN(p1); SBAR();      \
    N = MFMA(asbf(KN0), asbf(QN0), zero); SBAR();                                                                             \
    EX(S[4]); EX(S[5]); p0[0] = pk(S[0], S[1]); EX(S[6]); EX(S[7]); p0[1] = pk(S[2], S[3]); PIN(S); PIN(p0); SBAR();          \
    N = MFMA(asbf(KN1), asbf(QN1), N); SBAR();                                                                                \
    LD1;                                                                                                                      \
    EX(S[8]); EX(S[9]); p0[2] = pk(S[4], S[5]); EX(S[10]); EX(S[11]); p0[3] = pk(S[6], S[7]); PIN(S); PIN(p0); SBAR();        \
    OP = MFMA(asbf(VP), asbf(p1), OP); SBAR();                                                                                \
    EX(S[12]); EX(S[13]); p1[0] = pk(S[8], S[9]); EX(S[14]); EX(S[15]); p1[1] = pk(S[10], S[11]); PIN(S); PIN(p1); SBAR();    \
    OC = MFMA(asbf(VC), asbf(p0), OC); SBAR();                                                                                \
    LD2;                                                                                                                      \
  } while (0)
  for (int i = 1; i < n; i += 2) {
    const int tn = nxt(tc);
    SLOT(sa, sb, kA0, kA1, qb0, qb1, od, vB1, oa, vA0, (void)0, vB1 = KV(tv, 3));
    SLOT(sb, sa, kA0, kA1, qc0, qc1, oa, vA1, ob, vA0, (void)0, (void)0);
    SLOT(sa, sb, kA0, kA1, qd0, qd1, ob, vA1, oc, vA0, (kA0 = KV(tn, 0), kA1 = KV(tn, 1)), (void)0);
    SLOT(sb, sa, kB0, kB1, qa0, qa1, oc, vA1, od, vA0, (void)0, vA0 = KV(tn, 2));
    tc = nxt(tn);
    SLOT(sa, sb, kB0, kB1, qb0, qb1, od, vA1, oa, vB0, (void)0, vA1 = KV(tn, 3));
    SLOT(sb, sa, kB0, kB1, qc0, qc1, oa, vB1, ob, vB0, (void)0, (void)0);
    SLOT(sa, sb, kB0, kB1, qd0, qd1, ob, vB1, oc, vB0, (kB0 = KV(tc, 0), kB1 = KV(tc, 1)), (void)0);
    SLOT(sb, sa, kA0, kA1, qa0, qa1, oc, vB1, od, vB0, (void)0, vB0 = KV(tc, 2));
    tv = tc;
  }
  STAMP(4);
  BAR();
  f32x4* cw = (f32x4*)(lds + 114688) + lane;
#define PUT(O, J) do { const int slot = ((w + 1 + J) & 3) * 3 + J; _Pragma("unroll") for (int i = 0; i < 4; ++i) \
      cw[slot * 256 + i * 64] = (f32x4){O[4 * i], O[4 * i + 1], O[4 * i + 2], O[4 * i + 3]}; } while (0)
  SLOT(sa, sb, kA0, kA1, qb0, qb1, od, vB1, oa, vA0, (void)0, (void)0);
  SLOT(sb, sa, kA0, kA1, qc0, qc1, oa, vA1, ob, vA0, (void)0, (void)0);
  SLOT(sa, sb, kA0, kA1, qd0, qd1, ob, vA1, oc, vA0, PUT(oa, 0), (void)0);
  SLOT(sb, sa, kA0, kA1, qa0, qa1, oc, vA1, od, vA0, PUT(ob, 1), (void)0);
  p1[2] = pk(sb[12], sb[13]); p1[3] = pk(sb[14], sb[15]);
  od = MFMA(asbf(vA1), asbf(p1), od);
  PIN(oa); PIN(od); STAMP(2); CSTAMP(9);
  PUT(oc, 2);
  f32x16 o = od;
  __builtin_amdgcn_s_waitcnt(0xC07F);
  BAR();
  STAMP(3);
#pragma unroll
  for (int j = 0; j < 3; ++j)
#pragma unroll
    for (int i = 0; i < 4; ++i) { const f32x4 v = cw[(w * 3 + j) * 256 + i * 64]; o[4 * i] += v[0]; o[4 * i + 1] += v[1]; o[4 * i + 2] += v[2]; o[4 * i + 3] += v[3]; }
  float l;
  { auto rr = __builtin_amdgcn_permlane32_swap(__float_as_uint(o[12]), __float_as_uint(o[12]), false, false); l = __uint_as_float(rr[1]); }
  float amax = 0.f, nanchk = 0.f;
#pragma unroll
  for (int i = 0; i < 16; i += 2) { amax = fmaxf(fmaxf(amax, fabsf(o[i])), fabsf(o[i + 1])); nanchk += o[i] + o[i + 1]; }
  const bool ok = (l >= 1.0e-12f) && (l <= 1.0e12f) && (amax <= 1.0e37f) && (nanchk == nanchk);
  if (__builtin_expect(!__all(ok), 0)) {
    o = attn_slow(h, 4 * t + w, lane, ws);
    auto rr = __builtin_amdgcn_permlane32_swap(__float_as_uint(o[12]), __float_as_uint(o[12]), false, false); l = __uint_as_float(rr[1]);
  }
  o *= 1.f / l;
  { u32x4 o0 = pack8<0>(o), o1 = pack8<1>(o);
    if (hh == 1) { o1[2] = 0; o1[3] = 0; }
    u32x4* odst = (u32x4*)(ws + WS_O + (size_t)((4 * t + w) * 16 + h) * 2048) + lane;
    st16<16>(odst, o0); st16<16>(odst + 64, o1); }
  asm volatile("s_waitcnt vmcnt(0)" ::: "memory");
  if (lane == 0) set_flag(ws + WS_FLG + 1024 + (2 * t + (w >> 1)) * 128 + (2 * h + (w & 1)) * 4);
  STAMP(11); STAMP_OUT(1, w);
}

extern "C" void kernel_launch(void* const* d_in, const int* in_sizes, int n_in, void* d_out, int out_size, void* d_ws, size_t ws_size, hipStream_t stream) {
  char* ws = (char*)d_ws;
  k1_kernel<<<256, 512, 0, stream>>>((const char*)d_in[0], (const char*)d_in[4], (const char*)d_in[6], (const char*)d_in[1], (const char*)d_in[2], (const char*)d_in[5], ws);
  k2_kernel<<<256, 512, 0, stream>>>((const char*)d_in[5], ws, (char*)d_out);
}
```

```cpp
#include <hip/hip_runtime.h>
#include <stdint.h>

typedef short bf16x8 __attribute__((ext_vector_type(8)));
typedef short s16x4 __attribute__((ext_vector_type(4)));
typedef float f32x16 __attribute__((ext_vector_type(16)));
typedef float f32x4 __attribute__((ext_vector_type(4)));
typedef float f32x2 __attribute__((ext_vector_type(2)));
typedef unsigned u32x4 __attribute__((ext_vector_type(4)));
typedef __bf16 bf16x2_t __attribute__((ext_vector_type(2)));
#define K1_MAP 1
#define DI __device__ __forceinline__
#define LDS_AS __attribute__((address_space(3)))

namespace tab {
constexpr double PI = 3.14159265358979323846264338327950288;
constexpr double tsin(double x) { double x2 = x * x, t = x, s = x; for (int i = 1; i <= 10; ++i) { t *= -x2 / ((2 * i) * (2 * i + 1)); s += t; } return s; }
constexpr double tcos(double x) { double x2 = x * x, t = 1, s = 1; for (int i = 1; i <= 10; ++i) { t *= -x2 / ((2 * i - 1) * (2 * i)); s += t; } return s; }
constexpr double cosr(int m, int N) {
  m %= N; if (m < 0) m += N; if (2 * m > N) m = N - m;
  bool neg = false; if (4 * m > N) { m = N / 2 - m; neg = true; }
  double r = (8 * m > N) ? tsin(2 * PI * (N / 4 - m) / N) : tcos(2 * PI * m / N); return neg ? -r : r; }
constexpr double sinr(int m, int N) { return cosr(m - N / 4, N); }
constexpr uint32_t f2bf(double d) { float f = (float)d; uint32_t u = __builtin_bit_cast(uint32_t, f); u += 0x7fffu + ((u >> 16) & 1u); return u >> 16; }
constexpr int crow(int r, int hh) { return (r & 3) + 8 * (r >> 2) + 4 * hh; }
constexpr int krow(int s, int hh, int j) { return 16 * s + 8 * (j >> 2) + 4 * hh + (j & 3); }
constexpr int sigma(int r) { return 16 * ((r >> 2) & 1) + 4 * (r >> 3) + (r & 3); }
struct Base448 { uint16_t c[448], ns[448]; };
constexpr Base448 mk448() { Base448 b{}; for (int i = 0; i < 448; ++i) { b.c[i] = f2bf(cosr(i, 448)); b.ns[i] = f2bf(-sinr(i, 448)); } return b; }
constexpr Base448 B448 = mk448();
constexpr uint32_t Fval(int j, int c) { return c == 1 ? B448.c[(j * 224) % 448] : ((c & 1) ? B448.ns[(j * (c >> 1)) % 448] : B448.c[(j * (c >> 1)) % 448]); }
struct Tile { uint32_t v[28 * 64 * 4]; };
constexpr Tile mkF(int jt) { Tile t{}; for (int j = 0; j < 32; ++j) for (int c = 0; c < 224; ++c) t.v[j * 224 + c] = Fval(jt * 32 + j, 2 * c) | (Fval(jt * 32 + j, 2 * c + 1) << 16); return t; }
constexpr Tile mkG(int jt) { Tile t{}; for (int ks = 0; ks < 28; ++ks) for (int l = 0; l < 64; ++l) { int j = jt * 32 + sigma(l & 31), c0 = ks * 16 + 8 * (l >> 5);
  for (int e = 0; e < 4; ++e) t.v[(ks * 64 + l) * 4 + e] = Fval(j, c0 + 2 * e) | (Fval(j, c0 + 2 * e + 1) << 16); } return t; }
template <int T> constexpr Tile FT = mkF(T);
template <int T> constexpr Tile GT = mkG(T);
struct Tab14 { Tile t[14]; };
struct Fft { uint32_t ta[12][256]; float tw[8][256]; uint32_t tb[24][256]; };
struct Base2k { uint16_t c[2048]; };
constexpr Base2k mk2k() { Base2k b{}; for (int i = 0; i < 2048; ++i) b.c[i] = f2bf(cosr(i, 2048)); return b; }
constexpr Base2k B2K = mk2k();
constexpr uint32_t cs2k(int m, int which) { return B2K.c[(m + (which == 0 ? 0 : which == 1 ? 1536 : 512)) & 2047]; }
constexpr int fang(int set, int r, int k) {
  return set == 0 ? 64 * k * r : set == 1 ? (64 * k + 1) * r : set == 2 ? 32 * (2 * k + 1) * r : set == 3 ? 32 * (2 * r + 1) * k : set == 4 ? 64 * sigma(r) * k : (64 * sigma(r) + 1) * k; }
constexpr Fft mkFft() { Fft f{};
  for (int l = 0; l < 64; ++l) { const int r = l & 31, hh = l >> 5;
    for (int e = 0; e < 4; ++e) {
      for (int set = 0; set < 6; ++set) for (int w = 0; w < 3; ++w) for (int s = 0; s < 2; ++s) {
        const uint32_t v = cs2k(fang(set, r, krow(s, hh, 2 * e)), w) | (cs2k(fang(set, r, krow(s, hh, 2 * e + 1)), w) << 16);
        if (set < 2) f.ta[(set * 3 + w) * 2 + s][l * 4 + e] = v; else f.tb[((set - 2) * 3 + w) * 2 + s][l * 4 + e] = v; }
      for (int g = 0; g < 4; ++g) { const int m = crow(4 * g + e, hh) * r; f.tw[g][l * 4 + e] = (float)cosr(m, 1024); f.tw[4 + g][l * 4 + e] = (float)sinr(m, 1024); }
    } }
  return f; }
constexpr Fft FFT = mkFft();
}
#define TAB14(X) {{tab::X<0>, tab::X<1>, tab::X<2>, tab::X<3>, tab::X<4>, tab::X<5>, tab::X<6>, tab::X<7>, tab::X<8>, tab::X<9>, tab::X<10>, tab::X<11>, tab::X<12>, tab::X<13>}}
__device__ __attribute__((aligned(256))) const tab::Tab14 g_ftab = TAB14(FT);
__device__ __attribute__((aligned(256))) const tab::Tab14 g_gtab = TAB14(GT);
__device__ __attribute__((aligned(256))) const tab::Fft g_fft = tab::FFT;

constexpr size_t WS_Q = 0;
constexpr size_t WS_KV = WS_Q + (2u << 20);
constexpr size_t WS_X = WS_KV + (4u << 20);
constexpr size_t WS_WT = WS_X + (2u << 20);
constexpr size_t WS_G = WS_WT + (2u << 20);
constexpr size_t WS_FLG = WS_G + 65536;
constexpr size_t WS_O = WS_G + (1u << 20);
constexpr size_t WS_Y = WS_O + (2u << 20);

#define STAMP_DECL
#define STAMP(i)
#define STAMP_OUT(K, W)
#define CSTAMP(i)
#define MFMA(a, b, c) __builtin_amdgcn_mfma_f32_32x32x16_bf16((a), (b), (c), 0, 0, 0)
#define SBAR() __builtin_amdgcn_sched_barrier(0)
#define PIN(x) asm volatile("" : "+v"(x))
DI unsigned pk(float lo, float hi) { f32x2 v = {lo, hi}; return __builtin_bit_cast(unsigned, __builtin_convertvector(v, bf16x2_t)); }
DI bf16x8 asbf(u32x4 v) { return __builtin_bit_cast(bf16x8, v); }
template <int S> DI u32x4 pack8(const f32x16& a) { return (u32x4){pk(a[8 * S], a[8 * S + 1]), pk(a[8 * S + 2], a[8 * S + 3]), pk(a[8 * S + 4], a[8 * S + 5]), pk(a[8 * S + 6], a[8 * S + 7])}; }
DI float rbf(float v) { return __uint_as_float(pk(v, v) << 16); }
DI void glds(unsigned lds, unsigned voff, const void* sbase) {
  asm volatile("s_mov_b32 m0, %0\n\ts_nop 0\n\tglobal_load_lds_dwordx4 %1, %2" ::"s"(lds), "v"(voff), "s"(sbase) : "memory"); }
DI void glds4(unsigned lds, unsigned voff, const void* sbase) {
  asm volatile("s_mov_b32 m0, %0\n\ts_nop 0\n\tglobal_load_lds_dwordx4 %1, %2\n\tglobal_load_lds_dwordx4 %1, %2 offset:1024\n\t"
               "global_load_lds_dwordx4 %1, %2 offset:2048\n\tglobal_load_lds_dwordx4 %1, %2 offset:3072" ::"s"(lds), "v"(voff), "s"(sbase) : "memory"); }
DI void glds_dev(unsigned lds, unsigned voff, const void* sbase) {
  asm volatile("s_mov_b32 m0, %0\n\ts_nop 0\n\tglobal_load_lds_dwordx4 %1, %2 sc1" ::"s"(lds), "v"(voff), "s"(sbase) : "memory"); }
DI void glds4_dev(unsigned lds, unsigned voff, const void* sbase) {
  asm volatile("s_mov_b32 m0, %0\n\ts_nop 0\n\tglobal_load_lds_dwordx4 %1, %2 sc1\n\tglobal_load_lds_dwordx4 %1, %2 offset:1024 sc1\n\t"
               "global_load_lds_dwordx4 %1, %2 offset:2048 sc1\n\tglobal_load_lds_dwordx4 %1, %2 offset:3072 sc1" ::"s"(lds), "v"(voff), "s"(sbase) : "memory"); }
typedef __attribute__((address_space(1))) unsigned gu32;
DI void set_flag(char* p) { __hip_atomic_store((gu32*)p, 1u, __ATOMIC_RELAXED, __HIP_MEMORY_SCOPE_AGENT); }
template <int SLEEP> DI void wait_flags(const char* p) {
#pragma unroll 1
  for (int spin = 0; spin < (1 << 20); ++spin) {
    const unsigned v = __hip_atomic_load((gu32*)p, __ATOMIC_RELAXED, __HIP_MEMORY_SCOPE_AGENT);
    if (__all(v != 0u)) break;
    __builtin_amdgcn_s_sleep(SLEEP);
  }
  asm volatile("" ::: "memory");
}
DI void touch(unsigned lds, unsigned voff, const void* sbase) {
  asm volatile("s_mov_b32 m0, %0\n\ts_nop 0\n\tglobal_load_lds_dword %1, %2" ::"s"(lds), "v"(voff), "s"(sbase) : "memory"); }
#define AUX1 16
#define AUX2 16
#define AUX3 0
typedef unsigned v4u_t __attribute__((ext_vector_type(4)));
template <int AUX> DI void st16(void* p, u32x4 v) {
  if (AUX == 0) *(u32x4*)p = v;
  else if (AUX == 2) asm volatile("global_store_dwordx4 %0, %1, off nt\n\ts_nop 1" ::"v"(p), "v"(v) : "memory");
  else if (AUX == 16) asm volatile("global_store_dwordx4 %0, %1, off sc1\n\ts_nop 1" ::"v"(p), "v"(v) : "memory");
  else asm volatile("global_store_dwordx4 %0, %1, off sc0 sc1\n\ts_nop 1" ::"v"(p), "v"(v) : "memory");
}
#define MARK_SLEEP 32
#define CODE_LINES 210
#define ENDPGM() __builtin_amdgcn_endpgm()
typedef short v4i16_t __attribute__((ext_vector_type(4)));
DI bf16x8 lds_b128(unsigned a) { return *(const LDS_AS bf16x8*)(size_t)a; }
DI bf16x8 lds_tr2(unsigned a0, unsigned a1) {
  v4i16_t lo = __builtin_amdgcn_ds_read_tr16_b64_v4i16((LDS_AS v4i16_t*)(size_t)a0), hi = __builtin_amdgcn_ds_read_tr16_b64_v4i16((LDS_AS v4i16_t*)(size_t)a1);
  return __builtin_shufflevector(lo, hi, 0, 1, 2, 3, 4, 5, 6, 7); }
DI float halfmax(float m) { auto r = __builtin_amdgcn_permlane32_swap(__float_as_uint(m), __float_as_uint(m), false, false); return fmaxf(__uint_as_float(r[0]), __uint_as_float(r[1])); }
DI float max16(const f32x16& s) { float m = fmaxf(s[0], s[1]);
#pragma unroll
  for (int i = 2; i < 16; i += 2) m = fmaxf(fmaxf(m, s[i]), s[i + 1]);
  return m; }

constexpr int K1_SLOT = 32768;
struct K1Frag { bf16x8 x0, x1, w0, w1; };
struct K1Addr { unsigned x[4], w0, w1; };
DI void k1_read(K1Frag& f, const K1Addr& A, unsigned o, int ks) {
  f.x0 = lds_b128(A.x[ks] + o); f.w0 = lds_tr2(A.w0 + o + ks * 2048, A.w0 + o + ks * 2048 + 512);
  f.x1 = lds_b128(A.x[ks] + o + 4096); f.w1 = lds_tr2(A.w1 + o + ks * 2048, A.w1 + o + ks * 2048 + 512);
}
template <int TR> DI void k1_mma(const K1Frag& f, f32x16 (&acc)[2][2]) {
  if (TR) { acc[0][0] = MFMA(f.w0, f.x0, acc[0][0]); acc[1][0] = MFMA(f.w0, f.x1, acc[1][0]); acc[0][1] = MFMA(f.w1, f.x0, acc[0][1]); acc[1][1] = MFMA(f.w1, f.x1, acc[1][1]); }
  else    { acc[0][0] = MFMA(f.x0, f.w0, acc[0][0]); acc[1][0] = MFMA(f.x1, f.w0, acc[1][0]); acc[0][1] = MFMA(f.x0, f.w1, acc[0][1]); acc[1][1] = MFMA(f.x1, f.w1, acc[1][1]); }
}
#define K1_STAMP_ARG
#define K1_STAMP_PASS
template <int TR> DI void k1_compute(unsigned xa, unsigned wa, f32x16 (&acc)[2][2] K1_STAMP_ARG) {
  K1Addr A;
#pragma unroll
  for (int ks = 0; ks < 4; ++ks) A.x[ks] = xa ^ (ks * 32);
  A.w0 = wa; A.w1 = wa ^ 64;
  K1Frag fa, fb = {};
#define K1_RD(F, KS, M0, M1, M2, M3, G) do {                                                                                     \
    F.x0 = lds_b128(A.x[KS]); SBAR(); M0(G); SBAR();                                                                              \
    F.w0 = lds_tr2(A.w0 + KS * 2048, A.w0 + KS * 2048 + 512); if (KS == 3) { A.w0 += d; PIN(A.w0); } SBAR(); M1(G); SBAR();       \
    F.x1 = lds_b128(A.x[KS] + 4096); A.x[KS] += d; PIN(A.x[KS]); SBAR(); M2(G); SBAR();                                           \
    F.w1 = lds_tr2(A.w1 + KS * 2048, A.w1 + KS * 2048 + 512); if (KS == 3) { A.w1 += d; PIN(A.w1); } SBAR(); M3(G); SBAR(); } while (0)
#define K1_M0(G) acc[0][0] = TR ? MFMA(G.w0, G.x0, acc[0][0]) : MFMA(G.x0, G.w0, acc[0][0])
#define K1_M1(G) acc[1][0] = TR ? MFMA(G.w0, G.x1, acc[1][0]) : MFMA(G.x1, G.w0, acc[1][0])
#define K1_M2(G) acc[0][1] = TR ? MFMA(G.w1, G.x0, acc[0][1]) : MFMA(G.x0, G.w1, acc[0][1])
#define K1_M3(G) acc[1][1] = TR ? MFMA(G.w1, G.x1, acc[1][1]) : MFMA(G.x1, G.w1, acc[1][1])
  __builtin_amdgcn_s_waitcnt(0xC07F);
#pragma unroll 1
  for (int i = 0; i < 7; ++i) {
    asm volatile("s_barrier" ::: "memory");
    const unsigned d = i == 4 ? (unsigned)(-4 * K1_SLOT) : (unsigned)K1_SLOT;
    K1_RD(fa, 0, K1_M0, K1_M1, K1_M2, K1_M3, fb);
    K1_RD(fb, 1, K1_M0, K1_M1, K1_M2, K1_M3, fa);
    K1_RD(fa, 2, K1_M0, K1_M1, K1_M2, K1_M3, fb);
    K1_RD(fb, 3, K1_M0, K1_M1, K1_M2, K1_M3, fa);
  }
  k1_mma<TR>(fb, acc);
  PIN(acc[0][0]); PIN(acc[1][1]); STAMP(2); CSTAMP(9);
}

__global__ __launch_bounds__(512, 2) void k1_kernel(const char* __restrict__ x, const char* __restrict__ wqkv, const char* __restrict__ gw,
                                                     const char* __restrict__ wr, const char* __restrict__ wi, const char* __restrict__ wo, char* __restrict__ ws) {
  __shared__ __attribute__((aligned(1024))) char lds[5 * K1_SLOT];
  const int tid = threadIdx.x, lane = tid & 63, r = lane & 31, hh = lane >> 5;
  const int w8 = __builtin_amdgcn_readfirstlane(tid >> 6), w = w8 & 3;
  const int b = blockIdx.x, xcd = b & 7, bi = b >> 3;
  const unsigned l0 = (unsigned)(size_t)lds;
  STAMP_DECL; STAMP(0);
  const int t = 4 * (xcd >> 1) + (bi & 3), g = 7 * (xcd & 1) + (bi >> 2);
  if (bi >= 28 || w8 >= 4) {
  if (bi >= 28) {
    const int id = xcd * 4 + (bi - 28), fq = id >> 1, f1h = id & 1;
    {
      unsigned long long pc; unsigned rel;
      asm volatile("s_getpc_b64 %0\n\ts_mov_b32 %1, _Z9k1_kernelPKcS0_S0_S0_S0_S0_Pc@rel32@lo+4" : "=s"(pc), "=s"(rel));
      const unsigned ln = ((bi - 28) * 8 + w8) * 8 + (lane & 7);
      touch(l0 + 65536 + w8 * 256, (ln < CODE_LINES ? ln : 0) * 128u, (const char*)(pc + (long long)(int)rel)); }
    { const char* src = (w8 < 4 ? wr : wi) + (size_t)(128 * fq + 32 * w + 16 * f1h) * 450;
      const unsigned lb = l0 + w8 * 8192;
      glds4(lb, lane * 16u, src);
      glds(lb + 4096, lane * 16u, src + 4096); glds(lb + 5120, lane * 16u, src + 5120); glds(lb + 6144, lane * 16u, src + 6144);
      glds(lb + 7168, (lane < 2 ? lane : 0) * 16u, src + 7168); }
    const int gp = 64 * id + 8 * w8 + (lane >> 3);
    u32x4 gx[7], gv[7];
    { const unsigned xo = (unsigned)(64 * (gp & 31) + (gp >> 5)) * 896u + (lane & 7) * 16u, go = (lane & 7) * 16u;
#define GLDG(i) asm volatile("global_load_dwordx4 %0, %2, %4 offset:" #i "*128\n\tglobal_load_dwordx4 %1, %3, %5 offset:" #i "*128" : "=&v"(gx[i]), "=&v"(gv[i]) : "v"(xo), "v"(go), "s"(x), "s"(gw) : "memory")
      GLDG(0); GLDG(1); GLDG(2); GLDG(3); GLDG(4); GLDG(5); GLDG(6); }
    touch(l0 + 65536 + w8 * 256, (((bi - 28) * 8 + w8) * 11 + (lane < 11 ? lane : 0)) * 128u, &g_fft);
    st16<16>(ws + WS_O + (size_t)b * 8192 + tid * 16, (u32x4){~0u, ~0u, ~0u, ~0u});
    if (id == 0 && tid < 64) st16<16>(ws + WS_FLG + tid * 16, (u32x4){0u, 0u, 0u, 0u});
    asm volatile("s_waitcnt vmcnt(16)\n\ts_barrier" ::: "memory");
    { const int f1l = tid & 15, k0 = tid >> 4;
      const LDS_AS unsigned short* src = (const LDS_AS unsigned short*)(size_t)(l0 + f1l * 450 + k0 * 2);
      char* dst = ws + WS_WT + (size_t)k0 * 8192 + (size_t)(fq >> 1) * 1024 + (size_t)(32 * (fq & 1) + 16 * f1h + f1l) * 16;
      u32x4 v[8];
#pragma unroll
      for (int it = 0; it < 8; ++it)
#pragma unroll
        for (int e = 0; e < 4; ++e) v[it][e] = (unsigned)src[e * 4096 + it * 32] | ((unsigned)src[16384 + e * 4096 + it * 32] << 16);
      asm volatile("s_waitcnt vmcnt(2)" : "+v"(gx[0]), "+v"(gx[1]), "+v"(gx[2]), "+v"(gx[3]), "+v"(gx[4]), "+v"(gx[5]), "+v"(gx[6]),
                   "+v"(gv[0]), "+v"(gv[1]), "+v"(gv[2]), "+v"(gv[3]), "+v"(gv[4]), "+v"(gv[5]), "+v"(gv[6])::"memory");
#pragma unroll
      for (int it = 0; it < 8; ++it) if (it < 7 || k0 == 0) st16<AUX1>(dst + (size_t)it * 32 * 8192, v[it]);
    }
    { float z = 0.f;
#pragma unroll
      for (int i = 0; i < 7; ++i)
#pragma unroll
        for (int e = 0; e < 4; ++e) { const unsigned xa_ = gx[i][e], ga_ = gv[i][e]; z = __builtin_amdgcn_fdot2_f32_bf16(__builtin_bit_cast(bf16x2_t, xa_), __builtin_bit_cast(bf16x2_t, ga_), z, false); }
      z += __shfl_xor(z, 1); z += __shfl_xor(z, 2); z += __shfl_xor(z, 4);
      if ((lane & 7) == 0) *(float*)(ws + WS_G + (size_t)gp * 4) = rbf(1.f / (1.f + __expf(-rbf(z)))); }
    STAMP(9); STAMP(11); STAMP_OUT(0, w8);
    ENDPGM();
  }
  if (g >= 7 && g <= 10) {
    const int m0 = 128 * (g - 7);
    for (int hd = (m0 + 27 - 27) / 28 + w; hd < 16 && 28 * hd + 27 < m0 + 128; hd += 4) {
      if (28 * hd + 27 >= m0) {
        const unsigned pv = (lane & 3) == 0 ? 0x3F803F80u : 0u;
        st16<AUX1>(ws + WS_KV + (size_t)(hd * 64 + 4 * t + (lane >> 4)) * 4096 + 2048 + (size_t)((lane >> 2) & 3) * 512 + (size_t)(28 + (lane & 3)) * 16, (u32x4){pv, pv, pv, pv});
      }
    }
  }
  const int wr_ = w & 1, wc = w >> 1;
  f32x16 acc[2][2] = {};
  const int q = (lane & 15) >> 2, p = lane & 3, G = (lane >> 4) & 1;
  const unsigned xa = l0 + wr_ * 8192 + r * 128 + ((hh ^ ((r >> 1) & 7)) * 16);
  const unsigned wb = l0 + 16384 + wc * 8192 + (8 * hh + q) * 128 + (q >> 1) * 64;
  const int c0 = 128 * g + 64 * wc;
  if (g < 7) {
    k1_compute<1>(xa, wb + (4 * (p & 1) + 2 * G + (p >> 1)) * 8, acc K1_STAMP_PASS);
    STAMP(10);
    const bool isq = c0 < 448;
    const float sc = isq ? 0.1889822365046136f * 1.4426950408889634f : 1.f;
    const unsigned padv = isq ? 0u : 0x3F80u;
    char* base = ws + (isq ? WS_Q : WS_KV) + (size_t)r * 16;
    const unsigned hs = isq ? 131072u : 262144u, ts = isq ? 2048u : 4096u;
#pragma unroll
    for (int ct = 0; ct < 2; ++ct)
#pragma unroll
      for (int e = 0; e < 2; ++e) {
        const int g8 = (c0 - (isq ? 0 : 448)) / 8 + 4 * ct + 2 * hh + e;
        const int hp = (g8 * 37) >> 8, pos = g8 - 7 * hp;
        const unsigned off = (unsigned)(2 * hp + (pos > 3)) * hs + (unsigned)(pos > 3 ? pos - 3 : pos) * 512u;
#pragma unroll
        for (int rt = 0; rt < 2; ++rt) {
          const f32x16 v = acc[rt][ct] * sc;
          u32x4 d = e ? pack8<1>(v) : pack8<0>(v);
          char* dst = base + (size_t)(4 * t + 2 * wr_ + rt) * ts + off;
          if (pos == 3) { st16<AUX1>(dst + hs - 1536, (u32x4){padv, 0u, d[2], d[3]}); d[2] = padv; d[3] = 0u; }
          st16<AUX1>(dst, d);
        }
      }
  } else {
    k1_compute<0>(xa, wb + (4 * G + p) * 8, acc K1_STAMP_PASS);
    STAMP(10);
#pragma unroll
    for (int ct = 0; ct < 2; ++ct) {
      const int c = c0 + 32 * ct + r;
      char* dst; unsigned ts;
      if (c0 < 1344) { const int m = c - 896, hd = (m * 2341) >> 16, d = m - 28 * hd;
        dst = ws + WS_KV + (size_t)(hd * 64 + 4 * t + 2 * wr_) * 4096 + 2048 + (size_t)(d + 32 * hh) * 16; ts = 4096; }
      else { const int tile = 4 * t + 2 * wr_;
        dst = ws + WS_X + (size_t)(((c0 - 1344) >> 5) + ct) * 131072 + (size_t)tile * 2048 + (size_t)lane * 16; ts = 2048; }
#pragma unroll
      for (int rt = 0; rt < 2; ++rt) {
        st16<AUX1>(dst + rt * ts, pack8<0>(acc[rt][ct]));
        st16<AUX1>(dst + rt * ts + 1024, pack8<1>(acc[rt][ct]));
      }
    }
  }
  STAMP(11); STAMP_OUT(0, w8);
  ENDPGM();
  }
  {
    const int rot = (g + 2 * t) % 7;
    const unsigned xo_e = (unsigned)(lane >> 3) * (64u * 896u) + (unsigned)(((lane & 7) ^ (lane >> 4)) * 16);
    const unsigned xo_o = xo_e ^ 64u;
    const char* xb = x + (size_t)(4 * t + w) * 896;
    const int c0 = 128 * g + 64 * (w >> 1);
    const unsigned rs = c0 < 1344 ? 2688u : 896u;
    const char* cb = (c0 < 1344 ? wqkv + c0 * 2 : (const char*)&g_ftab + (c0 - 1344) * 2) + (size_t)(32 * (w & 1)) * rs;
    const unsigned co = (unsigned)(lane >> 3) * rs + (unsigned)(((lane & 7) ^ (4 * ((lane >> 4) & 1))) * 16);
    auto issue = [&](int i) __attribute__((always_inline)) {
      int c = i + rot; c = c >= 7 ? c - 7 : c;
      const unsigned lb = l0 + (i % 5) * K1_SLOT + w * 4096;
      const char* xc = xb + c * 128; const char* cc = cb + (size_t)(c * 64) * rs;
#pragma unroll
      for (int j = 0; j < 4; ++j) {
        glds(lb + j * 1024, (j & 1) ? xo_o : xo_e, xc + j * (8 * 64 * 896));
        glds(lb + 16384 + j * 1024, co, cc + j * 8 * rs);
      }
    };
    issue(0); issue(1);
    STAMP(1);
    asm volatile("s_waitcnt vmcnt(8)\n\ts_barrier" ::: "memory");
    STAMP(2);
    issue(2); issue(3);
    asm volatile("s_waitcnt vmcnt(16)\n\ts_barrier" ::: "memory");
    STAMP(3);
    issue(4);
    asm volatile("s_waitcnt vmcnt(16)\n\ts_barrier" ::: "memory");
    STAMP(4);
    issue(5);
    asm volatile("s_waitcnt vmcnt(16)\n\ts_barrier" ::: "memory");
    STAMP(5);
    issue(6);
    st16<16>(ws + WS_O + (size_t)b * 8192 + tid * 16, (u32x4){~0u, ~0u, ~0u, ~0u});
    st16<16>(ws + WS_O + (size_t)b * 8192 + 4096 + tid * 16, (u32x4){~0u, ~0u, ~0u, ~0u});
    asm volatile("s_waitcnt vmcnt(16)\n\ts_barrier" ::: "memory");
    STAMP(6);
    { const unsigned lo = ((bi * 4 + w) * 28 + (lane < 28 ? lane : 0)) * 128u; touch(l0 + 2 * K1_SLOT + w * 256, lo, wo); touch(l0 + 2 * K1_SLOT + w * 256, lo, &g_gtab); }
    asm volatile("s_waitcnt vmcnt(10)\n\ts_barrier" ::: "memory");
    STAMP(7);
    asm volatile("s_waitcnt vmcnt(2)\n\ts_barrier" ::: "memory");
    STAMP(8);
    STAMP(11); STAMP_OUT(0, w8);
  }
}

#define EX(x) x = __builtin_amdgcn_exp2f(x)
DI f32x16 lds_f16(unsigned a) { f32x16 v;
#pragma unroll
  for (int g = 0; g < 4; ++g) { const f32x4 c = *(const LDS_AS f32x4*)(size_t)(a + g * 1024);
#pragma unroll
    for (int e = 0; e < 4; ++e) v[4 * g + e] = c[e]; }
  return v; }
#define FSTAMP(i)
#define FFT_GAP 0
DI f32x16 FMFMA(bf16x8 a, bf16x8 b, f32x16 c) {
  f32x16 d = MFMA(a, b, c);
  if (FFT_GAP == 1) { SBAR(); __builtin_amdgcn_s_sleep(1); SBAR(); }
  return d; }
template <int SG, bool DA> DI void cmm(f32x16& re, f32x16& im, const u32x4 (&dr)[2], const u32x4 (&di)[2], const bf16x8 (&T)[6]) {
  re = (f32x16){}; im = (f32x16){};
#define CM(D, TT, ACC) ACC = DA ? FMFMA(asbf(D), TT, ACC) : FMFMA(TT, asbf(D), ACC)
#pragma unroll
  for (int s = 0; s < 2; ++s) {
    CM(dr[s], T[s], re); CM(di[s], T[s], im);
    CM(di[s], T[(SG < 0 ? 2 : 4) + s], re); CM(dr[s], T[(SG < 0 ? 4 : 2) + s], im);
  }
}
DI void pack2(u32x4 (&d)[2], const f32x16& v) { d[0] = pack8<0>(v); d[1] = pack8<1>(v); }
DI void fft_wave(int u, int lane, char* __restrict__ ws, unsigned l0 K1_STAMP_ARG) {
  const bool special = (u == 0) | (u == 224);
  const int cr = u == 224 ? 1 : 2 * u;
  const unsigned vo = lane * 16u;
  const char* ts = (const char*)&g_fft;
  { const char* xs = ws + WS_X + (size_t)(cr >> 5) * 131072 + (size_t)(cr & 31) * 16;
    const unsigned xo = (unsigned)(lane >> 1) * 4096u + (unsigned)(lane & 1) * 16u;
#pragma unroll
    for (int i = 0; i < 8; ++i) glds(l0 + i * 1024, xo, xs + i * 512);
#pragma unroll 1
    for (int i = 0; i < 5; ++i) glds4(l0 + 8192 + i * 4096, vo, ts + i * 4096); }
  unsigned L = l0 + vo, LX = l0 + (lane >> 5) * 1024 + (lane & 31) * 32; PIN(L); PIN(LX);
#define FR(i) lds_b128(L + (i) * 1024)
#define FT6(T, i) _Pragma("unroll") for (int j_ = 0; j_ < 6; ++j_) T[j_] = FR((i) + j_)
  FSTAMP(1);
  asm volatile("s_waitcnt vmcnt(0)" ::: "memory");
  FSTAMP(2);
  { const char* wsrc = ws + WS_WT + (size_t)u * 8192;
    glds4(l0 + 52 * 1024, vo, wsrc); glds4(l0 + 56 * 1024, vo, wsrc + 4096); }
  const f32x16 twc = lds_f16(L + 20 * 1024), tws = lds_f16(L + 24 * 1024);
  bf16x8 D[6], T[6];
  FT6(D, 8); FT6(T, 14);
  u32x4 ar[2][2], ai[2][2];
  f32x16 re, im;
#pragma unroll
  for (int tau = 0; tau < 2; ++tau) {
    u32x4 zr[2], zi[2];
#pragma unroll
    for (int s = 0; s < 2; ++s) { zr[s] = __builtin_bit_cast(u32x4, lds_b128(LX + tau * 4096 + s * 2048)); zi[s] = __builtin_bit_cast(u32x4, lds_b128(LX + tau * 4096 + s * 2048 + 16)); }
    if (special) { zi[0] = (u32x4){0, 0, 0, 0}; zi[1] = zi[0]; }
    SBAR();
    if (tau == 0) cmm<-1, true>(re, im, zr, zi, D); else cmm<-1, true>(re, im, zr, zi, T);
    glds4(l0 + (28 + tau * 12) * 1024, vo, ts + (20 + tau * 12) * 1024); glds4(l0 + (32 + tau * 12) * 1024, vo, ts + (24 + tau * 12) * 1024); glds4(l0 + (36 + tau * 12) * 1024, vo, ts + (28 + tau * 12) * 1024);
    pack2(ar[tau], re * twc + im * tws); pack2(ai[tau], im * twc - re * tws);
    SBAR();
  }
  FSTAMP(3);
  asm volatile("s_waitcnt vmcnt(12)" ::: "memory");
  FSTAMP(4);
  u32x4 ur[2][2], ui[2][2];
  {
    FT6(T, 28);
    u32x4 wv[8];
#pragma unroll
    for (int g = 0; g < 8; ++g) wv[g] = __builtin_bit_cast(u32x4, FR(52 + g));
    SBAR();
    f32x16 ore, oim;
    cmm<-1, false>(re, im, ar[0], ai[0], D);
    cmm<-1, false>(ore, oim, ar[1], ai[1], T);
    f32x16 wr_, wi_;
#define UNP(B) _Pragma("unroll") for (int g = 0; g < 4; ++g) _Pragma("unroll") for (int e = 0; e < 4; ++e) { wr_[4 * g + e] = __uint_as_float(wv[B + g][e] << 16); wi_[4 * g + e] = __uint_as_float(wv[B + g][e] & 0xffff0000u); }
    UNP(4);
    const f32x16 z1r = re - ore, z1i = im - oim;
    const f32x16 u1r = z1r * wr_ - z1i * wi_, u1i = z1r * wi_ + z1i * wr_;
    UNP(0);
    const f32x16 z0r = re + ore, z0i = im + oim;
    const f32x16 u0r = z0r * wr_ - z0i * wi_, u0i = z0r * wi_ + z0i * wr_;
    pack2(ur[0], u0r + u1r); pack2(ui[0], u0i + u1i); pack2(ur[1], u0r - u1r); pack2(ui[1], u0i - u1i);
    SBAR();
  }
  FSTAMP(5);
  asm volatile("s_waitcnt vmcnt(0)" ::: "memory");
  FSTAMP(6);
  const float osc = special ? 0.5f : 1.f;
  FT6(T, 34);
#pragma unroll
  for (int kap = 0; kap < 2; ++kap) {
    bf16x8 T4[6];
    FT6(T4, 40 + kap * 6);
    SBAR();
    if (kap == 0) cmm<1, true>(re, im, ur[0], ui[0], D); else cmm<1, true>(re, im, ur[1], ui[1], T);
    u32x4 br[2], bi[2];
    pack2(br, re * twc - im * tws); pack2(bi, im * twc + re * tws);
    cmm<1, false>(re, im, br, bi, T4);
    re *= osc;
    { const unsigned ya = l0 + (lane & 31) * 128 + kap * 64 + (lane >> 5) * 32;
      *(LDS_AS u32x4*)(size_t)ya = pack8<0>(re); *(LDS_AS u32x4*)(size_t)(ya + 16) = pack8<1>(re);
      *(LDS_AS u32x4*)(size_t)(ya + 4096) = pack8<0>(im); *(LDS_AS u32x4*)(size_t)(ya + 4096 + 16) = pack8<1>(im); }
    SBAR();
  }
  { char* yd = ws + WS_Y + (size_t)cr * 4096 + vo;
    u32x4 v[8];
#pragma unroll
    for (int i = 0; i < 8; ++i) v[i] = __builtin_bit_cast(u32x4, FR(i));
#pragma unroll
    for (int i = 0; i < 4; ++i) st16<16>(yd + i * 1024, v[i]);
    if (!special) {
#pragma unroll
      for (int i = 4; i < 8; ++i) st16<16>(yd + i * 1024, v[i]); } }
}

DI f32x16 attn_slow(int h, int tile, int lane, const char* __restrict__ ws) {
  const u32x4* qp = (const u32x4*)(ws + WS_Q + (size_t)(h * 64 + tile) * 2048) + lane;
  const u32x4 q0 = qp[0], q1 = qp[64];
  const u32x4* kv = (const u32x4*)(ws + WS_KV + (size_t)h * 262144) + lane;
  float m = -INFINITY;
#pragma unroll 1
  for (int i = 0; i < 64; ++i) {
    f32x16 s = {}; s = MFMA(asbf(kv[i * 256]), asbf(q0), s); s = MFMA(asbf(kv[i * 256 + 64]), asbf(q1), s);
    m = fmaxf(m, max16(s));
  }
  m = halfmax(m);
  f32x16 o = {};
#pragma unroll 1
  for (int i = 0; i < 64; ++i) {
    f32x16 s = {}; s = MFMA(asbf(kv[i * 256]), asbf(q0), s); s = MFMA(asbf(kv[i * 256 + 64]), asbf(q1), s);
#pragma unroll
    for (int j = 0; j < 16; ++j) s[j] = __builtin_amdgcn_exp2f(s[j] - m);
    o = MFMA(asbf(kv[i * 256 + 128]), asbf(pack8<0>(s)), o); o = MFMA(asbf(kv[i * 256 + 192]), asbf(pack8<1>(s)), o);
  }
  return o;
}

#define FFT_PRIO 3
#define FFT_SLEEP 8
#define N0 13
#define N1 17
#define N2 17
#define GLD(dst, voff, sb, imm) asm volatile("global_load_dwordx4 %0, %1, %2 offset:" #imm : "=v"(dst) : "v"(voff), "s"(sb) : "memory")
#define BAR() asm volatile("s_barrier" ::: "memory")
__global__ __launch_bounds__(512, 2) void k2_kernel(const char* __restrict__ wo, char* __restrict__ ws, char* __restrict__ out) {
  __shared__ __attribute__((aligned(1024))) char lds[163840];
  const int tid = threadIdx.x, lane = tid & 63, hh = lane >> 5;
  const int w8 = __builtin_amdgcn_readfirstlane(tid >> 6), w = w8 & 3;
  const int b = blockIdx.x, xcd = b & 7, bi = b >> 3;
  const int h = 2 * xcd + (bi & 1), t = bi >> 1;
  const unsigned l0 = (unsigned)(size_t)lds;
  const unsigned vo = lane * 16u;
  STAMP_DECL; STAMP(0);
  if (w8 >= 4) {
    const int rb = 4 * xcd + bi / 7, cb = bi % 7, rt = w >> 1, ct = w & 1, r = lane & 31;
    const unsigned sw = (unsigned)(((lane & 7) ^ (4 * ((lane >> 4) & 1))) * 16);
    const int u = 32 * xcd + bi;
    if (w == 0) {
      __builtin_amdgcn_s_sleep(FFT_SLEEP);
      __builtin_amdgcn_s_setprio(FFT_PRIO);
      if (u < 225) fft_wave(u, lane, ws, l0 + 102400 K1_STAMP_PASS);
      asm volatile("s_waitcnt vmcnt(0)" ::: "memory");
      if (lane == 0) set_flag(ws + WS_FLG + u * 4);
    } else {
      __builtin_amdgcn_s_sleep(100);
      if (bi < 28) { const unsigned so = (unsigned)(lane >> 3) * 896u + sw; const char* sb = wo + cb * 128;
#pragma unroll 1
        for (int j = w - 1; j < 56; j += 3) { glds(l0 + j * 1024, so, sb + (size_t)j * 8 * 896); __builtin_amdgcn_s_sleep(2); } }
    }
    STAMP(1);
    if (bi >= 28) { STAMP(11); STAMP_OUT(1, w8); ENDPGM(); }
    u32x4 f[32];
    float g;
    { const char* db = (const char*)&g_gtab + (size_t)(2 * cb + ct) * 28672;
#define GLD4(I) do { const char* d0 = db + (I) * 1024; GLD(f[I], vo, d0, 0); GLD(f[I + 1], vo, d0, 1024); GLD(f[I + 2], vo, d0, 2048); GLD(f[I + 3], vo, d0, 3072); __builtin_amdgcn_s_sleep(2); } while (0)
      GLD4(0); GLD4(4); GLD4(8); GLD4(12); GLD4(16); GLD4(20); GLD4(24);
      asm volatile("global_load_dword %0, %1, %2" : "=v"(g) : "v"(r * 4u), "s"(ws + WS_G + (size_t)(rb * 64 + rt * 32) * 4) : "memory"); }
    __builtin_amdgcn_s_setprio(0);
#define F8(I) "+v"(f[I]), "+v"(f[I + 1]), "+v"(f[I + 2]), "+v"(f[I + 3]), "+v"(f[I + 4]), "+v"(f[I + 5]), "+v"(f[I + 6])
    STAMP(2);
    BAR();
    if (w == 1) {
#pragma unroll 1
      for (int spin = 0; spin < (1 << 20); ++spin) {
        u32x4 v; asm volatile("global_load_dwordx4 %0, %1, %2 sc1\n\ts_waitcnt vmcnt(0)" : "=&v"(v) : "v"(vo), "s"(ws + WS_FLG) : "memory");
        if (__all((v[0] & v[1] & v[2] & v[3]) != 0u)) break;
        __builtin_amdgcn_s_sleep(64);
      } }
    STAMP(3);
    BAR();
    { const unsigned so = (unsigned)(lane >> 3) * 4096u + sw; const char* sb = ws + WS_Y + rb * 128 + (size_t)w * 14 * 8 * 4096;
#pragma unroll
      for (int j = 0; j < 14; ++j) glds_dev(l0 + 57344 + (w * 14 + j) * 1024, so, sb + (size_t)j * 8 * 4096); }
    BAR();
    asm volatile("s_waitcnt vmcnt(0)" : F8(0), F8(7), F8(14), F8(21), "+v"(g)::"memory");
    STAMP(4);
    BAR();
    STAMP(5);
    const int q = (lane & 15) >> 2, p = lane & 3, G = (lane >> 4) & 1;
    f32x16 acca = {}, accb = {};
    { unsigned ya = l0 + 57344 + (8 * hh + q) * 128 + (((q >> 1) ^ rt) * 64) + G * 32 + p * 8; PIN(ya);
      bf16x8 y[4];
#pragma unroll
      for (int i = 0; i < 4; ++i) y[i] = lds_tr2(ya + i * 2048, ya + i * 2048 + 512);
#pragma unroll
      for (int i = 0; i < 28; ++i) { SBAR(); acca = MFMA(asbf(f[i]), y[i & 3], acca); SBAR(); if (i + 4 < 28) y[i & 3] = lds_tr2(ya + (i + 4) * 2048, ya + (i + 4) * 2048 + 512); } }
    SBAR();
    STAMP(6);
    if (w == 0) {
#pragma unroll 1
      for (int spin = 0; spin < (1 << 20); ++spin) {
        unsigned v; asm volatile("global_load_dword %0, %1, %2 sc1\n\ts_waitcnt vmcnt(0)" : "=&v"(v) : "v"(lane * 1024u + 1008u - 512u), "s"(ws + WS_O + (size_t)rb * 65536) : "memory");
        if (!__any(v == ~0u)) break;
        __builtin_amdgcn_s_sleep(1);
      } }
    BAR();
    STAMP(8);
    { const char* ob_ = ws + WS_O + (size_t)(2 * rb + rt) * 32768 + ct * 8192; const unsigned lb = l0 + 57344 + rt * 16384 + ct * 8192;
      glds4_dev(lb, vo, ob_); glds4_dev(lb + 4096, vo, ob_ + 4096);
      glds4_dev(lb + 32768, vo, ob_ + 16384); glds4_dev(lb + 32768 + 4096, vo, ob_ + 16384 + 4096); }
    unsigned w0 = l0 + (4 * hh + q) * 128 + (((q >> 1) ^ ct) * 64) + (4 * (p & 1) + 2 * G + (p >> 1)) * 8, w2 = w0 - hh * 512; PIN(w0); PIN(w2);
#pragma unroll
    for (int j = 0; j < 32; ++j) { const unsigned o_ = (j >> 1) * 3584 + (j & 1) * 2048; f[j] = __builtin_bit_cast(u32x4, lds_tr2(w0 + o_, ((j & 1) ? w2 : w0) + o_ + 1024)); }
    acca *= g * (2.0f / (2048.f * 448.f));
    const float g1 = rbf(1.f - g);
    unsigned oa_ = l0 + 57344 + rt * 16384 + vo; PIN(oa_);
    unsigned mx = 0;
#define MX4(Y) do { const u32x4 y_ = __builtin_bit_cast(u32x4, Y); mx = max(max(mx, y_[0]), y_[1]); mx = max(max(mx, y_[2]), y_[3]); PIN(mx); } while (0)
    __builtin_amdgcn_s_waitcnt(0xC07F);
#pragma unroll
    for (int hf = 0; hf < 2; ++hf) {
      if (hf == 0) asm volatile("s_waitcnt vmcnt(8)\n\ts_barrier" ::: "memory"); else asm volatile("s_waitcnt vmcnt(0)\n\ts_barrier" ::: "memory");
      STAMP(7 + 3 * hf);
      bf16x8 y[4];
#pragma unroll
      for (int i = 0; i < 4; ++i) y[i] = lds_b128(oa_ + hf * 32768 + i * 1024);
#pragma unroll
      for (int i = 0; i < 16; ++i) { SBAR(); accb = MFMA(asbf(f[hf * 16 + i]), y[i & 3], accb); MX4(y[i & 3]); SBAR(); if (i + 4 < 16) y[i & 3] = lds_b128(oa_ + hf * 32768 + (i + 4) * 1024); }
    }
    if (__builtin_expect(__any(mx == ~0u), 0)) {
      accb = (f32x16){};
      const char* ob_ = ws + WS_O + (size_t)(2 * rb + rt) * 32768;
#pragma unroll 1
      for (int j = 0; j < 32; ++j) {
        u32x4 v;
#pragma unroll 1
        for (int spin = 0; spin < (1 << 20); ++spin) {
          asm volatile("global_load_dwordx4 %0, %1, %2 sc1\n\ts_waitcnt vmcnt(0)" : "=&v"(v) : "v"(vo), "s"(ob_ + j * 1024) : "memory");
          if (!__any(max(max(v[0], v[1]), max(v[2], v[3])) == ~0u)) break;
        }
        const unsigned o_ = (j >> 1) * 3584 + (j & 1) * 2048;
        accb = MFMA(lds_tr2(w0 + o_, ((j & 1) ? w2 : w0) + o_ + 1024), asbf(v), accb);
      }
    }
    const f32x16 res = accb * g1 + acca;
    u32x4* od_ = (u32x4*)(out + (size_t)(64 * r + 2 * rb + rt) * 896 + (size_t)(cb * 64 + ct * 32 + 16 * hh) * 2);
    st16<AUX3>(od_, pack8<0>(res)); st16<AUX3>(od_ + 1, pack8<1>(res));
    STAMP(11); STAMP_OUT(1, w8);
    ENDPGM();
  }
  const int t0 = w == 0 ? 0 : w == 1 ? N0 : w == 2 ? N0 + N1 : N0 + N1 + N2;
  const int t1 = w == 0 ? N0 : w == 1 ? N0 + N1 : w == 2 ? N0 + N1 + N2 : 64;
  const int n = t1 - t0;
  int tc = t0 + ((t * n) >> 4);
  auto nxt = [&](int v) __attribute__((always_inline)) { return v + 1 == t1 ? t0 : v + 1; };
  const auto kvr = __builtin_amdgcn_make_buffer_rsrc((void*)(ws + WS_KV + (size_t)h * 262144), 0, 262144, 0x00020000);
#define KV(tile, idx) __builtin_bit_cast(u32x4, __builtin_amdgcn_raw_buffer_load_b128(kvr, vo + (idx) * 1024, (tile) * 4096, 0))
  const u32x4* qp = (const u32x4*)(ws + WS_Q + (size_t)(h * 64 + 4 * t) * 2048) + lane;
  const u32x4 kr0 = KV(4 * t, 0), kr1 = KV(4 * t, 1);
  const int ia = ((w + 1) & 3) * 128, ib = ((w + 2) & 3) * 128, ic = ((w + 3) & 3) * 128, id_ = w * 128;
  u32x4 qa0 = qp[ia], qa1 = qp[ia + 64], qb0 = qp[ib], qb1 = qp[ib + 64], qc0 = qp[ic], qc1 = qp[ic + 64], qd0 = qp[id_], qd1 = qp[id_ + 64];
  SBAR();
  u32x4 kA0 = KV(tc, 0), kA1 = KV(tc, 1); SBAR();
  u32x4 vA0 = KV(tc, 2); SBAR();
  u32x4 vA1 = KV(tc, 3); SBAR();
  int tv = nxt(tc);
  u32x4 kB0 = KV(tv, 0); SBAR(); u32x4 kB1 = KV(tv, 1); SBAR();
  u32x4 vB0 = KV(tv, 2), vB1 = {0, 0, 0, 0}; SBAR();
  tc = tv;
  f32x16 oa = {}, ob = {}, oc = {}, od = {}, sa, sb;
  const f32x16 zero = {};
  u32x4 p0, p1 = {0, 0, 0, 0};
  PIN(oa); PIN(ob); PIN(oc); PIN(od); PIN(p1); PIN(vB1); SBAR();
  {
    const bool selA = (h & 1) && hh == 0, selB = !(h & 1) && hh == 1;
    f32x16 s0 = MFMA(asbf(kr0), asbf(qa0), zero); s0 = MFMA(asbf(kr1), asbf(qa1), s0);
    f32x16 s1 = MFMA(asbf(kr0), asbf(qb0), zero); s1 = MFMA(asbf(kr1), asbf(qb1), s1);
    f32x16 s2 = MFMA(asbf(kr0), asbf(qc0), zero); s2 = MFMA(asbf(kr1), asbf(qc1), s2);
    f32x16 s3 = MFMA(asbf(kr0), asbf(qd0), zero); s3 = MFMA(asbf(kr1), asbf(qd1), s3);
#define REF(S, Q0, Q1) do { const unsigned nm = pk(-max16(S), 0.f); Q0[0] = selA ? nm : Q0[0]; Q1[2] = selB ? nm : Q1[2]; } while (0)
    REF(s0, qa0, qa1);
    sa = MFMA(asbf(kA0), asbf(qa0), zero); sa = MFMA(asbf(kA1), asbf(qa1), sa);
    REF(s1, qb0, qb1); REF(s2, qc0, qc1); REF(s3, qd0, qd1);
  }
  sb = zero;
  PIN(sa); STAMP(1); CSTAMP(8);
#define SLOT(S, N, KN0, KN1, QN0, QN1, OP, VP, OC, VC, LD1, LD2)                                                              \
  do {                                                                                                                        \
    SBAR();                                                                                                                   \
    EX(S[0]); EX(S[1]); p1[2] = pk(N[12], N[13]); EX(S[2]); EX(S[3]); p1[3] = pk(N[14], N[15]); PIN(S); PIN(p1); SBAR();      \
    N = MFMA(asbf(KN0), asbf(QN0), zero); PIN(N); SBAR();                                                                             \
    EX(S[4]); EX(S[5]); p0[0] = pk(S[0], S[1]); EX(S[6]); EX(S[7]); p0[1] = pk(S[2], S[3]); PIN(S); PIN(p0); SBAR();          \
    N = MFMA(asbf(KN1), asbf(QN1), N); PIN(N); SBAR();                                                                                \
    LD1;                                                                                                                      \
    EX(S[8]); EX(S[9]); p0[2] = pk(S[4], S[5]); EX(S[10]); EX(S[11]); p0[3] = pk(S[6], S[7]); PIN(S); PIN(p0); SBAR();        \
    OP = MFMA(asbf(VP), asbf(p1), OP); PIN(OP); SBAR();                                                                                \
    EX(S[12]); EX(S[13]); p1[0] = pk(S[8], S[9]); EX(S[14]); EX(S[15]); p1[1] = pk(S[10], S[11]); PIN(S); PIN(p1); SBAR();    \
    OC = MFMA(asbf(VC), asbf(p0), OC); PIN(OC); SBAR();                                                                                \
    LD2;                                                                                                                      \
  } while (0)
#define ITS(k)
  unsigned ca = l0 + 114688 + vo + ((w + 1) & 3) * 12288, cb_ = l0 + 114688 + vo + ((w + 2) & 3) * 12288 + 4096, cc = l0 + 114688 + vo + ((w + 3) & 3) * 12288 + 8192, cr_ = l0 + 114688 + vo + w * 12288;
  PIN(ca); PIN(cb_); PIN(cc); PIN(cr_);
  int i = 1;
  do {
    ITS(i - 1);
    const int tn = nxt(tc);
    if (i == n - 2) BAR();
    SLOT(sa, sb, kA0, kA1, qb0, qb1, od, vB1, oa, vA0, (void)0, vB1 = KV(tv, 3));
    SLOT(sb, sa, kA0, kA1, qc0, qc1, oa, vA1, ob, vA0, (void)0, (void)0);
    SLOT(sa, sb, kA0, kA1, qd0, qd1, ob, vA1, oc, vA0, (kA0 = KV(tn, 0), kA1 = KV(tn, 1)), (void)0);
    SLOT(sb, sa, kB0, kB1, qa0, qa1, oc, vA1, od, vA0, (void)0, vA0 = KV(tn, 2));
    tc = nxt(tn);
    ITS(i);
    SLOT(sa, sb, kB0, kB1, qb0, qb1, od, vA1, oa, vB0, (void)0, vA1 = KV(tn, 3));
    SLOT(sb, sa, kB0, kB1, qc0, qc1, oa, vB1, ob, vB0, (void)0, (void)0);
    SLOT(sa, sb, kB0, kB1, qd0, qd1, ob, vB1, oc, vB0, (kB0 = KV(tc, 0), kB1 = KV(tc, 1)), (void)0);
    SLOT(sb, sa, kA0, kA1, qa0, qa1, oc, vB1, od, vB0, (void)0, vB0 = KV(tc, 2));
    tv = tc;
    i += 2;
  } while (i < n);
  ITS(n - 1);
  STAMP(4);
  BAR();
#define PUT2(O, A, I) do { *(LDS_AS f32x4*)(size_t)(A + (I) * 1024) = (f32x4){O[4 * (I)], O[4 * (I) + 1], O[4 * (I) + 2], O[4 * (I) + 3]}; \
                           *(LDS_AS f32x4*)(size_t)(A + (I) * 1024 + 1024) = (f32x4){O[4 * (I) + 4], O[4 * (I) + 5], O[4 * (I) + 6], O[4 * (I) + 7]}; } while (0)
  SLOT(sa, sb, kA0, kA1, qb0, qb1, od, vB1, oa, vA0, (void)0, (void)0);
  SLOT(sb, sa, kA0, kA1, qc0, qc1, oa, vA1, ob, vA0, (void)0, (void)0);
  SLOT(sa, sb, kA0, kA1, qd0, qd1, ob, vA1, oc, vA0, PUT2(oa, ca, 0), PUT2(oa, ca, 2));
  SBAR();
  EX(sb[0]); EX(sb[1]); p1[2] = pk(sa[12], sa[13]); EX(sb[2]); EX(sb[3]); p1[3] = pk(sa[14], sa[15]); PIN(sb); PIN(p1); SBAR();
  oc = MFMA(asbf(vA1), asbf(p1), oc); PIN(oc); SBAR();
  EX(sb[4]); EX(sb[5]); p0[0] = pk(sb[0], sb[1]); EX(sb[6]); EX(sb[7]); p0[1] = pk(sb[2], sb[3]); PIN(sb); PIN(p0); SBAR();
  PUT2(ob, cb_, 0); SBAR();
  EX(sb[8]); EX(sb[9]); p0[2] = pk(sb[4], sb[5]); EX(sb[10]); EX(sb[11]); p0[3] = pk(sb[6], sb[7]); PIN(sb); PIN(p0); SBAR();
  PUT2(ob, cb_, 2); SBAR();
  od = MFMA(asbf(vA0), asbf(p0), od); PIN(od); SBAR();
  EX(sb[12]); EX(sb[13]); p1[0] = pk(sb[8], sb[9]); EX(sb[14]); EX(sb[15]); p1[1] = pk(sb[10], sb[11]); PIN(sb); PIN(p1); SBAR();
  PUT2(oc, cc, 0); SBAR();
  p1[2] = pk(sb[12], sb[13]); p1[3] = pk(sb[14], sb[15]);
  PUT2(oc, cc, 2); SBAR();
  od = MFMA(asbf(vA1), asbf(p1), od);
  PIN(od); STAMP(2); CSTAMP(9);
  f32x16 o = od;
  __builtin_amdgcn_s_waitcnt(0xC07F);
  BAR();
  STAMP(3);
#pragma unroll
  for (int j = 0; j < 12; ++j) { const f32x4 v = *(const LDS_AS f32x4*)(size_t)(cr_ + j * 1024); const int e = 4 * (j & 3); o[e] += v[0]; o[e + 1] += v[1]; o[e + 2] += v[2]; o[e + 3] += v[3]; }
  float l;
  { auto rr = __builtin_amdgcn_permlane32_swap(__float_as_uint(o[12]), __float_as_uint(o[12]), false, false); l = __uint_as_float(rr[1]); }
  f32x2 sm = (f32x2){o[0], o[1]};
#pragma unroll
  for (int j = 2; j < 16; j += 2) sm += (f32x2){o[j], o[j + 1]};
  const bool ok = (l >= 1.0e-12f) && (l <= 1.0e12f) && (fabsf(sm[0] + sm[1]) <= 3.0e38f);
  if (__builtin_expect(!__all(ok), 0)) {
    o = attn_slow(h, 4 * t + w, lane, ws);
    auto rr = __builtin_amdgcn_permlane32_swap(__float_as_uint(o[12]), __float_as_uint(o[12]), false, false); l = __uint_as_float(rr[1]);
  }
  o *= __builtin_amdgcn_rcpf(l);
  { u32x4 o0 = pack8<0>(o), o1 = pack8<1>(o);
    if (hh == 1) { o1[2] = 0; o1[3] = 0; }
    u32x4* odst = (u32x4*)(ws + WS_O + (size_t)((4 * t + w) * 16 + h) * 2048) + lane;
#pragma unroll
    for (int i = 0; i < 4; ++i) { o0[i] = min(o0[i], 0xFFFFFFFEu); o1[i] = min(o1[i], 0xFFFFFFFEu); }
    st16<16>(odst, o0); st16<16>(odst + 64, o1); }
  STAMP(11);
  BAR();
  STAMP_OUT(1, w);
}

extern "C" void kernel_launch(void* const* d_in, const int* in_sizes, int n_in, void* d_out, int out_size, void* d_ws, size_t ws_size, hipStream_t stream) {
  char* ws = (char*)d_ws;
  k1_kernel<<<256, 512, 0, stream>>>((const char*)d_in[0], (const char*)d_in[4], (const char*)d_in[6], (const char*)d_in[1], (const char*)d_in[2], (const char*)d_in[5], ws);
  k2_kernel<<<256, 512, 0, stream>>>((const char*)d_in[5], ws, (char*)d_out);
}
```

```cpp
#include <hip/hip_runtime.h>
#include <stdint.h>

typedef short bf16x8 __attribute__((ext_vector_type(8)));
typedef short s16x4 __attribute__((ext_vector_type(4)));
typedef float f32x16 __attribute__((ext_vector_type(16)));
typedef float f32x4 __attribute__((ext_vector_type(4)));
typedef float f32x2 __attribute__((ext_vector_type(2)));
typedef unsigned u32x4 __attribute__((ext_vector_type(4)));
typedef __bf16 bf16x2_t __attribute__((ext_vector_type(2)));
#define K1_MAP 1
#define DI __device__ __forceinline__
#define LDS_AS __attribute__((address_space(3)))

namespace tab {
constexpr double PI = 3.14159265358979323846264338327950288;
constexpr double tsin(double x) { double x2 = x * x, t = x, s = x; for (int i = 1; i <= 10; ++i) { t *= -x2 / ((2 * i) * (2 * i + 1)); s += t; } return s; }
constexpr double tcos(double x) { double x2 = x * x, t = 1, s = 1; for (int i = 1; i <= 10; ++i) { t *= -x2 / ((2 * i - 1) * (2 * i)); s += t; } return s; }
constexpr double cosr(int m, int N) {
  m %= N; if (m < 0) m += N; if (2 * m > N) m = N - m;
  bool neg = false; if (4 * m > N) { m = N / 2 - m; neg = true; }
  double r = (8 * m > N) ? tsin(2 * PI * (N / 4 - m) / N) : tcos(2 * PI * m / N); return neg ? -r : r; }
constexpr double sinr(int m, int N) { return cosr(m - N / 4, N); }
constexpr uint32_t f2bf(double d) { float f = (float)d; uint32_t u = __builtin_bit_cast(uint32_t, f); u += 0x7fffu + ((u >> 16) & 1u); return u >> 16; }
constexpr int crow(int r, int hh) { return (r & 3) + 8 * (r >> 2) + 4 * hh; }
constexpr int krow(int s, int hh, int j) { return 16 * s + 8 * (j >> 2) + 4 * hh + (j & 3); }
constexpr int sigma(int r) { return 16 * ((r >> 2) & 1) + 4 * (r >> 3) + (r & 3); }
struct Base448 { uint16_t c[448], ns[448]; };
constexpr Base448 mk448() { Base448 b{}; for (int i = 0; i < 448; ++i) { b.c[i] = f2bf(cosr(i, 448)); b.ns[i] = f2bf(-sinr(i, 448)); } return b; }
constexpr Base448 B448 = mk448();
constexpr uint32_t Fval(int j, int c) { return c == 1 ? B448.c[(j * 224) % 448] : ((c & 1) ? B448.ns[(j * (c >> 1)) % 448] : B448.c[(j * (c >> 1)) % 448]); }
struct Tile { uint32_t v[28 * 64 * 4]; };
constexpr Tile mkF(int jt) { Tile t{}; for (int j = 0; j < 32; ++j) for (int c = 0; c < 224; ++c) t.v[j * 224 + c] = Fval(jt * 32 + j, 2 * c) | (Fval(jt * 32 + j, 2 * c + 1) << 16); return t; }
constexpr Tile mkG(int jt) { Tile t{}; for (int ks = 0; ks < 28; ++ks) for (int l = 0; l < 64; ++l) { int j = jt * 32 + sigma(l & 31), c0 = ks * 16 + 8 * (l >> 5);
  for (int e = 0; e < 4; ++e) t.v[(ks * 64 + l) * 4 + e] = Fval(j, c0 + 2 * e) | (Fval(j, c0 + 2 * e + 1) << 16); } return t; }
template <int T> constexpr Tile FT = mkF(T);
template <int T> constexpr Tile GT = mkG(T);
struct Tab14 { Tile t[14]; };
struct Fft { uint32_t ta[12][256]; float tw[8][256]; uint32_t tb[24][256]; };
struct Base2k { uint16_t c[2048]; };
constexpr Base2k mk2k() { Base2k b{}; for (int i = 0; i < 2048; ++i) b.c[i] = f2bf(cosr(i, 2048)); return b; }
constexpr Base2k B2K = mk2k();
constexpr uint32_t cs2k(int m, int which) { return B2K.c[(m + (which == 0 ? 0 : which == 1 ? 1536 : 512)) & 2047]; }
constexpr int fang(int set, int r, int k) {
  return set == 0 ? 64 * k * r : set == 1 ? (64 * k + 1) * r : set == 2 ? 32 * (2 * k + 1) * r : set == 3 ? 32 * (2 * r + 1) * k : set == 4 ? 64 * sigma(r) * k : (64 * sigma(r) + 1) * k; }
constexpr Fft mkFft() { Fft f{};
  for (int l = 0; l < 64; ++l) { const int r = l & 31, hh = l >> 5;
    for (int e = 0; e < 4; ++e) {
      for (int set = 0; set < 6; ++set) for (int w = 0; w < 3; ++w) for (int s = 0; s < 2; ++s) {
        const uint32_t v = cs2k(fang(set, r, krow(s, hh, 2 * e)), w) | (cs2k(fang(set, r, krow(s, hh, 2 * e + 1)), w) << 16);
        if (set < 2) f.ta[(set * 3 + w) * 2 + s][l * 4 + e] = v; else f.tb[((set - 2) * 3 + w) * 2 + s][l * 4 + e] = v; }
      for (int g = 0; g < 4; ++g) { const int m = crow(4 * g + e, hh) * r; f.tw[g][l * 4 + e] = (float)cosr(m, 1024); f.tw[4 + g][l * 4 + e] = (float)sinr(m, 1024); }
    } }
  return f; }
constexpr Fft FFT = mkFft();
}
#define TAB14(X) {{tab::X<0>, tab::X<1>, tab::X<2>, tab::X<3>, tab::X<4>, tab::X<5>, tab::X<6>, tab::X<7>, tab::X<8>, tab::X<9>, tab::X<10>, tab::X<11>, tab::X<12>, tab::X<13>}}
__device__ __attribute__((aligned(256))) const tab::Tab14 g_ftab = TAB14(FT);
__device__ __attribute__((aligned(256))) const tab::Tab14 g_gtab = TAB14(GT);
__device__ __attribute__((aligned(256))) const tab::Fft g_fft = tab::FFT;

constexpr size_t WS_Q = 0;
constexpr size_t WS_KV = WS_Q + (2u << 20);
constexpr size_t WS_X = WS_KV + (4u << 20);
constexpr size_t WS_WT = WS_X + (2u << 20);
constexpr size_t WS_G = WS_WT + (2u << 20);
constexpr size_t WS_FLG = WS_G + 65536;
constexpr size_t WS_O = WS_G + (1u << 20);
constexpr size_t WS_Y = WS_O + (2u << 20);

#define STAMP_DECL
#define STAMP(i)
#define STAMP_OUT(K, W)
#define CSTAMP(i)
#define MFMA(a, b, c) __builtin_amdgcn_mfma_f32_32x32x16_bf16((a), (b), (c), 0, 0, 0)
#define SBAR() __builtin_amdgcn_sched_barrier(0)
#define PIN(x) asm volatile("" : "+v"(x))
DI unsigned pk(float lo, float hi) { f32x2 v = {lo, hi}; return __builtin_bit_cast(unsigned, __builtin_convertvector(v, bf16x2_t)); }
DI bf16x8 asbf(u32x4 v) { return __builtin_bit_cast(bf16x8, v); }
template <int S> DI u32x4 pack8(const f32x16& a) { return (u32x4){pk(a[8 * S], a[8 * S + 1]), pk(a[8 * S + 2], a[8 * S + 3]), pk(a[8 * S + 4], a[8 * S + 5]), pk(a[8 * S + 6], a[8 * S + 7])}; }
DI float rbf(float v) { return __uint_as_float(pk(v, v) << 16); }
DI void glds(unsigned lds, unsigned voff, const void* sbase) {
  asm volatile("s_mov_b32 m0, %0\n\ts_nop 0\n\tglobal_load_lds_dwordx4 %1, %2" ::"s"(lds), "v"(voff), "s"(sbase) : "memory"); }
DI void glds4(unsigned lds, unsigned voff, const void* sbase) {
  asm volatile("s_mov_b32 m0, %0\n\ts_nop 0\n\tglobal_load_lds_dwordx4 %1, %2\n\tglobal_load_lds_dwordx4 %1, %2 offset:1024\n\t"
               "global_load_lds_dwordx4 %1, %2 offset:2048\n\tglobal_load_lds_dwordx4 %1, %2 offset:3072" ::"s"(lds), "v"(voff), "s"(sbase) : "memory"); }
DI void glds_dev(unsigned lds, unsigned voff, const void* sbase) {
  asm volatile("s_mov_b32 m0, %0\n\ts_nop 0\n\tglobal_load_lds_dwordx4 %1, %2 sc1" ::"s"(lds), "v"(voff), "s"(sbase) : "memory"); }
DI void glds4_dev(unsigned lds, unsigned voff, const void* sbase) {
  asm volatile("s_mov_b32 m0, %0\n\ts_nop 0\n\tglobal_load_lds_dwordx4 %1, %2 sc1\n\tglobal_load_lds_dwordx4 %1, %2 offset:1024 sc1\n\t"
               "global_load_lds_dwordx4 %1, %2 offset:2048 sc1\n\tglobal_load_lds_dwordx4 %1, %2 offset:3072 sc1" ::"s"(lds), "v"(voff), "s"(sbase) : "memory"); }
typedef __attribute__((address_space(1))) unsigned gu32;
DI void set_flag(char* p) { __hip_atomic_store((gu32*)p, 1u, __ATOMIC_RELAXED, __HIP_MEMORY_SCOPE_AGENT); }
template <int SLEEP> DI void wait_flags(const char* p) {
#pragma unroll 1
  for (int spin = 0; spin < (1 << 20); ++spin) {
    const unsigned v = __hip_atomic_load((gu32*)p, __ATOMIC_RELAXED, __HIP_MEMORY_SCOPE_AGENT);
    if (__all(v != 0u)) break;
    __builtin_amdgcn_s_sleep(SLEEP);
  }
  asm volatile("" ::: "memory");
}
DI void touch(unsigned lds, unsigned voff, const void* sbase) {
  asm volatile("s_mov_b32 m0, %0\n\ts_nop 0\n\tglobal_load_lds_dword %1, %2" ::"s"(lds), "v"(voff), "s"(sbase) : "memory"); }
#define AUX1 16
#define AUX2 16
#define AUX3 0
typedef unsigned v4u_t __attribute__((ext_vector_type(4)));
template <int AUX> DI void st16(void* p, u32x4 v) {
  if (AUX == 0) *(u32x4*)p = v;
  else if (AUX == 2) asm volatile("global_store_dwordx4 %0, %1, off nt\n\ts_nop 1" ::"v"(p), "v"(v) : "memory");
  else if (AUX == 16) asm volatile("global_store_dwordx4 %0, %1, off sc1\n\ts_nop 1" ::"v"(p), "v"(v) : "memory");
  else asm volatile("global_store_dwordx4 %0, %1, off sc0 sc1\n\ts_nop 1" ::"v"(p), "v"(v) : "memory");
}
#define MARK_SLEEP 32
#define CODE_LINES 218
#define A_SLEEP 10
#define ASTAMP(i)
#define ENDPGM() __builtin_amdgcn_endpgm()
typedef short v4i16_t __attribute__((ext_vector_type(4)));
DI bf16x8 lds_b128(unsigned a) { return *(const LDS_AS bf16x8*)(size_t)a; }
DI bf16x8 lds_tr2(unsigned a0, unsigned a1) {
  v4i16_t lo = __builtin_amdgcn_ds_read_tr16_b64_v4i16((LDS_AS v4i16_t*)(size_t)a0), hi = __builtin_amdgcn_ds_read_tr16_b64_v4i16((LDS_AS v4i16_t*)(size_t)a1);
  return __builtin_shufflevector(lo, hi, 0, 1, 2, 3, 4, 5, 6, 7); }
DI float halfmax(float m) { auto r = __builtin_amdgcn_permlane32_swap(__float_as_uint(m), __float_as_uint(m), false, false); return fmaxf(__uint_as_float(r[0]), __uint_as_float(r[1])); }
DI float max16(const f32x16& s) { float m = fmaxf(s[0], s[1]);
#pragma unroll
  for (int i = 2; i < 16; i += 2) m = fmaxf(fmaxf(m, s[i]), s[i + 1]);
  return m; }

constexpr int K1_SLOT = 32768;
struct K1Frag { bf16x8 x0, x1, w0, w1; };
struct K1Addr { unsigned x[4], w0, w1; };
DI void k1_read(K1Frag& f, const K1Addr& A, unsigned o, int ks) {
  f.x0 = lds_b128(A.x[ks] + o); f.w0 = lds_tr2(A.w0 + o + ks * 2048, A.w0 + o + ks * 2048 + 512);
  f.x1 = lds_b128(A.x[ks] + o + 4096); f.w1 = lds_tr2(A.w1 + o + ks * 2048, A.w1 + o + ks * 2048 + 512);
}
template <int TR> DI void k1_mma(const K1Frag& f, f32x16 (&acc)[2][2]) {
  if (TR) { acc[0][0] = MFMA(f.w0, f.x0, acc[0][0]); acc[1][0] = MFMA(f.w0, f.x1, acc[1][0]); acc[0][1] = MFMA(f.w1, f.x0, acc[0][1]); acc[1][1] = MFMA(f.w1, f.x1, acc[1][1]); }
  else    { acc[0][0] = MFMA(f.x0, f.w0, acc[0][0]); acc[1][0] = MFMA(f.x1, f.w0, acc[1][0]); acc[0][1] = MFMA(f.x0, f.w1, acc[0][1]); acc[1][1] = MFMA(f.x1, f.w1, acc[1][1]); }
}
#define K1_STAMP_ARG
#define K1_STAMP_PASS
template <int TR> DI void k1_compute(unsigned xa, unsigned wa, f32x16 (&acc)[2][2] K1_STAMP_ARG) {
  K1Addr A;
#pragma unroll
  for (int ks = 0; ks < 4; ++ks) A.x[ks] = xa ^ (ks * 32);
  A.w0 = wa; A.w1 = wa ^ 64;
  K1Frag fa, fb = {};
#define K1_RD(F, KS, M0, M1, M2, M3, G) do {                                                                                     \
    F.x0 = lds_b128(A.x[KS]); SBAR(); M0(G); SBAR();                                                                              \
    F.w0 = lds_tr2(A.w0 + KS * 2048, A.w0 + KS * 2048 + 512); if (KS == 3) { A.w0 += d; PIN(A.w0); } SBAR(); M1(G); SBAR();       \
    F.x1 = lds_b128(A.x[KS] + 4096); A.x[KS] += d; PIN(A.x[KS]); SBAR(); M2(G); SBAR();                                           \
    F.w1 = lds_tr2(A.w1 + KS * 2048, A.w1 + KS * 2048 + 512); if (KS == 3) { A.w1 += d; PIN(A.w1); } SBAR(); M3(G); SBAR(); } while (0)
#define K1_M0(G) acc[0][0] = TR ? MFMA(G.w0, G.x0, acc[0][0]) : MFMA(G.x0, G.w0, acc[0][0])
#define K1_M1(G) acc[1][0] = TR ? MFMA(G.w0, G.x1, acc[1][0]) : MFMA(G.x1, G.w0, acc[1][0])
#define K1_M2(G) acc[0][1] = TR ? MFMA(G.w1, G.x0, acc[0][1]) : MFMA(G.x0, G.w1, acc[0][1])
#define K1_M3(G) acc[1][1] = TR ? MFMA(G.w1, G.x1, acc[1][1]) : MFMA(G.x1, G.w1, acc[1][1])
  PIN(acc[0][0]); PIN(acc[1][1]); STAMP(3);
  asm volatile("s_waitcnt vmcnt(0)" ::: "memory");
  __builtin_amdgcn_s_waitcnt(0xC07F);
#pragma unroll 1
  for (int i = 0; i < 7; ++i) {
    asm volatile("s_barrier" ::: "memory");
    const unsigned d = i == 4 ? (unsigned)(-4 * K1_SLOT) : (unsigned)K1_SLOT;
    K1_RD(fa, 0, K1_M0, K1_M1, K1_M2, K1_M3, fb);
    K1_RD(fb, 1, K1_M0, K1_M1, K1_M2, K1_M3, fa);
    K1_RD(fa, 2, K1_M0, K1_M1, K1_M2, K1_M3, fb);
    K1_RD(fb, 3, K1_M0, K1_M1, K1_M2, K1_M3, fa);
  }
  k1_mma<TR>(fb, acc);
  PIN(acc[0][0]); PIN(acc[1][1]); STAMP(2); CSTAMP(9);
}

__global__ __launch_bounds__(512, 2) void k1_kernel(const char* __restrict__ x, const char* __restrict__ wqkv, const char* __restrict__ gw,
                                                     const char* __restrict__ wr, const char* __restrict__ wi, const char* __restrict__ wo, char* __restrict__ ws) {
  __shared__ __attribute__((aligned(1024))) char lds[5 * K1_SLOT];
  const int tid = threadIdx.x, lane = tid & 63, r = lane & 31, hh = lane >> 5;
  const int w8 = __builtin_amdgcn_readfirstlane(tid >> 6), w = w8 & 3;
  const int b = blockIdx.x, xcd = b & 7, bi = b >> 3;
  const unsigned l0 = (unsigned)(size_t)lds;
  STAMP_DECL; STAMP(0);
  if (bi < 2 && w8 >= 4) {
    unsigned long long pc; unsigned rel;
    asm volatile("s_getpc_b64 %0\n\ts_mov_b32 %1, _Z9k1_kernelPKcS0_S0_S0_S0_S0_Pc@rel32@lo+4" : "=s"(pc), "=s"(rel));
    const unsigned ln = (bi * 4 + w) * 32 + r;
    touch(l0 + 4 * K1_SLOT + w * 256, (ln < CODE_LINES ? ln : 0) * 128u, (const char*)(pc + (long long)(int)rel));
  }
  const int t = 4 * (xcd >> 1) + (bi & 3), g = 7 * (xcd & 1) + (bi >> 2);
  const bool aux = bi >= 28;
  int rot = (bi >> 2) + 2 * (bi & 3); rot = rot >= 7 ? rot - 7 : rot;
  const unsigned xo = (unsigned)(lane >> 3) * (64u * 896u) + (unsigned)(((lane & 7) ^ (lane >> 4)) * 16) + (unsigned)(4 * t + w) * 896u;
#define XLD(SLOT, C) do { const unsigned v_ = xo + (C) * 128u; const unsigned l_ = l0 + (SLOT) * K1_SLOT + w * 4096;                     \
    glds(l_, v_, x); glds(l_ + 1024, (v_ ^ 64u) + 458752u, x); glds(l_ + 2048, v_ + 917504u, x); glds(l_ + 3072, (v_ ^ 64u) + 1376256u, x); } while (0)
  if (!aux && w8 < 4) XLD(0, rot);
  const int lc0 = 128 * g + 64 * (w >> 1);
  const unsigned rs = lc0 < 1344 ? 2688u : 896u;
  const char* cb = lc0 < 1344 ? wqkv : (const char*)&g_ftab - 2688;
  const unsigned co = (unsigned)((lane >> 3) + 32 * (w & 1)) * rs + (unsigned)(((lane & 7) ^ (4 * ((lane >> 4) & 1))) * 16) + lc0 * 2u;
#define BLD(SLOT, C) do { const unsigned v_ = co + (C) * 64u * rs; const unsigned l_ = l0 + (SLOT) * K1_SLOT + 16384 + w * 4096;          \
    glds(l_, v_, cb); glds(l_ + 1024, v_ + 8u * rs, cb); glds(l_ + 2048, v_ + 16u * rs, cb); glds(l_ + 3072, v_ + 24u * rs, cb); } while (0)
  if (!aux && w8 < 4) BLD(0, rot);
  if (aux) {
    const char* seg = (w8 < 4 ? wr : wi) + (size_t)(64 * (xcd * 4 + (bi & 2)) + 32 * w + 16 * (bi & 1)) * 450;
    glds4(l0 + w8 * 8192, lane * 16u, seg); glds4(l0 + w8 * 8192 + 3104, lane * 16u, seg + 3104);
  }
  STAMP(9);
  if (bi >= 28 || w8 >= 4) {
  if (bi >= 28) {
    const int id = xcd * 4 + (bi - 28), fq = id >> 1, f1h = id & 1;
    touch(l0 + 65536 + w8 * 256, (((bi - 28) * 8 + w8) * 11 + (lane < 11 ? lane : 0)) * 128u, &g_fft);
    ASTAMP(1);
    asm volatile("s_waitcnt vmcnt(1)\n\ts_barrier" ::: "memory");
    ASTAMP(2);
    st16<16>(ws + WS_O + (size_t)b * 8192 + tid * 16, (u32x4){~0u, ~0u, ~0u, ~0u});
    if (id == 0 && tid < 64) st16<16>(ws + WS_FLG + tid * 16, (u32x4){0u, 0u, 0u, 0u});
    const int gp = 64 * id + 8 * w8 + (lane >> 3);
    u32x4 gx[7], gv[7];
    { const unsigned xo = (unsigned)(64 * (gp & 31) + (gp >> 5)) * 896u + (lane & 7) * 16u, go = (lane & 7) * 16u;
#define GLDG(i) asm volatile("global_load_dwordx4 %0, %2, %4 offset:" #i "*128\n\tglobal_load_dwordx4 %1, %3, %5 offset:" #i "*128" : "=&v"(gx[i]), "=&v"(gv[i]) : "v"(xo), "v"(go), "s"(x), "s"(gw) : "memory")
      GLDG(0); GLDG(1); GLDG(2); GLDG(3); GLDG(4); GLDG(5); GLDG(6); }
    ASTAMP(3);
    { const int f1l = tid & 15, k0 = tid >> 4;
      const LDS_AS unsigned short* src = (const LDS_AS unsigned short*)(size_t)(l0 + f1l * 450 + k0 * 2);
      char* dst = ws + WS_WT + (size_t)k0 * 8192 + (size_t)(fq >> 1) * 1024 + (size_t)(32 * (fq & 1) + 16 * f1h + f1l) * 16;
      u32x4 v[8];
#pragma unroll
      for (int it = 0; it < 8; ++it)
#pragma unroll
        for (int e = 0; e < 4; ++e) v[it][e] = (unsigned)src[e * 4096 + it * 32] | ((unsigned)src[16384 + e * 4096 + it * 32] << 16);
      ASTAMP(4);
#pragma unroll
      for (int it = 0; it < 8; ++it) if (it < 7 || k0 == 0) st16<AUX1>(dst + (size_t)it * 32 * 8192, v[it]);
      ASTAMP(5);
      asm volatile("s_waitcnt vmcnt(7)" : "+v"(gx[0]), "+v"(gx[1]), "+v"(gx[2]), "+v"(gx[3]), "+v"(gx[4]), "+v"(gx[5]), "+v"(gx[6]),
                   "+v"(gv[0]), "+v"(gv[1]), "+v"(gv[2]), "+v"(gv[3]), "+v"(gv[4]), "+v"(gv[5]), "+v"(gv[6])::"memory");
    }
    ASTAMP(6);
    { float z = 0.f;
#pragma unroll
      for (int i = 0; i < 7; ++i)
#pragma unroll
        for (int e = 0; e < 4; ++e) { const unsigned xa_ = gx[i][e], ga_ = gv[i][e]; z = __builtin_amdgcn_fdot2_f32_bf16(__builtin_bit_cast(bf16x2_t, xa_), __builtin_bit_cast(bf16x2_t, ga_), z, false); }
      z += __shfl_xor(z, 1); z += __shfl_xor(z, 2); z += __shfl_xor(z, 4);
      if ((lane & 7) == 0) *(float*)(ws + WS_G + (size_t)gp * 4) = rbf(1.f / (1.f + __expf(-rbf(z)))); }
    STAMP(11);
    STAMP_OUT(0, w8);
    ENDPGM();
  }
  const int wr_ = w & 1, wc = w >> 1;
  f32x16 acc[2][2] = {};
  const int q = (lane & 15) >> 2, p = lane & 3, G = (lane >> 4) & 1;
  const unsigned xa = l0 + wr_ * 8192 + r * 128 + ((hh ^ ((r >> 1) & 7)) * 16);
  const unsigned wb = l0 + 16384 + wc * 8192 + (8 * hh + q) * 128 + (q >> 1) * 64;
  const int c0 = 128 * g + 64 * wc;
  const auto wsr = __builtin_amdgcn_make_buffer_rsrc((void*)ws, 0, (int)WS_WT, 0x00020000);
#define BST(V, VOFF) __builtin_amdgcn_raw_buffer_store_b128((V), wsr, (VOFF), 0, AUX1)
  const unsigned tile0 = 4 * t + 2 * wr_;
  if (g < 7) {
    const bool isq = c0 < 448;
    const unsigned padv = isq ? 0u : 0x3F80u;
    const unsigned hs = isq ? 131072u : 262144u, ts = isq ? 2048u : 4096u;
    unsigned off[4], of2[4];
#pragma unroll
    for (int j = 0; j < 4; ++j) {
      const int g8 = (c0 - (isq ? 0 : 448)) / 8 + 4 * (j >> 1) + 2 * hh + (j & 1);
      const int hp = (g8 * 37) >> 8, pos = g8 - 7 * hp;
      off[j] = (isq ? (unsigned)WS_Q : (unsigned)WS_KV) + tile0 * ts + r * 16u + (unsigned)(2 * hp + (pos > 3)) * hs + (unsigned)(pos > 3 ? pos - 3 : pos) * 512u;
      of2[j] = pos == 3 ? off[j] + hs - 1536u : 0x80000000u;
      PIN(off[j]); PIN(of2[j]);
    }
    k1_compute<1>(xa, wb + (4 * (p & 1) + 2 * G + (p >> 1)) * 8, acc K1_STAMP_PASS);
    STAMP(10);
    if (isq) {
#pragma unroll
      for (int j = 0; j < 4; ++j) acc[j >> 1][j & 1] *= 0.1889822365046136f * 1.4426950408889634f; }
#pragma unroll
    for (int j = 0; j < 4; ++j) {
      const bool bn = of2[j] != 0x80000000u;
      u32x4 d0 = (j & 1) ? pack8<1>(acc[0][j >> 1]) : pack8<0>(acc[0][j >> 1]), d1 = (j & 1) ? pack8<1>(acc[1][j >> 1]) : pack8<0>(acc[1][j >> 1]);
      if (__any(bn)) {
        BST(((u32x4){padv, 0u, d0[2], d0[3]}), of2[j]); BST(((u32x4){padv, 0u, d1[2], d1[3]}), of2[j] + ts);
        d0[2] = bn ? padv : d0[2]; d0[3] = bn ? 0u : d0[3]; d1[2] = bn ? padv : d1[2]; d1[3] = bn ? 0u : d1[3]; }
      BST(d0, off[j]); BST(d1, off[j] + ts);
    }
  } else {
    unsigned off[2]; const unsigned ts = c0 < 1344 ? 4096u : 2048u;
#pragma unroll
    for (int ct = 0; ct < 2; ++ct) {
      if (c0 < 1344) { const int m = c0 + 32 * ct + r - 896, hd = (m * 2341) >> 16, d = m - 28 * hd; off[ct] = (unsigned)WS_KV + hd * 262144u + 2048u + (unsigned)(d + 32 * hh) * 16u; }
      else off[ct] = (unsigned)WS_X + (unsigned)(((c0 - 1344) >> 5) + ct) * 131072u + lane * 16u;
      off[ct] += tile0 * ts; PIN(off[ct]);
    }
    k1_compute<0>(xa, wb + (4 * G + p) * 8, acc K1_STAMP_PASS);
    STAMP(10);
#pragma unroll
    for (int ct = 0; ct < 2; ++ct)
#pragma unroll
      for (int rt = 0; rt < 2; ++rt) { BST(pack8<0>(acc[rt][ct]), off[ct] + rt * ts); BST(pack8<1>(acc[rt][ct]), off[ct] + rt * ts + 1024u); }
  }
  STAMP(11); STAMP_OUT(0, w8);
  ENDPGM();
  }
  {
#define issue(I) do { int c_ = (I) + rot; c_ = c_ >= 7 ? c_ - 7 : c_; XLD((I) % 5, c_); BLD((I) % 5, c_); } while (0)
    issue(1);
    STAMP(1);
    asm volatile("s_waitcnt vmcnt(8)\n\ts_barrier" ::: "memory");
    STAMP(2);
    issue(2);
    asm volatile("s_waitcnt vmcnt(8)\n\ts_barrier" ::: "memory");
    STAMP(3);
    issue(3); issue(4);
    asm volatile("s_waitcnt vmcnt(16)\n\ts_barrier" ::: "memory");
    STAMP(4);
    issue(5);
    asm volatile("s_waitcnt vmcnt(16)\n\ts_barrier" ::: "memory");
    STAMP(5);
    issue(6);
    st16<16>(ws + WS_O + (size_t)b * 8192 + tid * 16, (u32x4){~0u, ~0u, ~0u, ~0u});
    st16<16>(ws + WS_O + (size_t)b * 8192 + 4096 + tid * 16, (u32x4){~0u, ~0u, ~0u, ~0u});
    asm volatile("s_waitcnt vmcnt(16)\n\ts_barrier" ::: "memory");
    STAMP(6);
    { const unsigned lo = ((bi * 4 + w) * 28 + (lane < 28 ? lane : 0)) * 128u; touch(l0 + 2 * K1_SLOT + w * 256, lo, wo); touch(l0 + 2 * K1_SLOT + w * 256, lo, &g_gtab); }
    asm volatile("s_waitcnt vmcnt(10)\n\ts_barrier" ::: "memory");
    STAMP(7);
    asm volatile("s_waitcnt vmcnt(2)\n\ts_barrier" ::: "memory");
    STAMP(8);
  if (g >= 7 && g <= 10) {
    const int m0 = 128 * (g - 7);
    for (int hd = (m0 + 27 - 27) / 28 + w; hd < 16 && 28 * hd + 27 < m0 + 128; hd += 4) {
      if (28 * hd + 27 >= m0) {
        const unsigned pv = (lane & 3) == 0 ? 0x3F803F80u : 0u;
        st16<AUX1>(ws + WS_KV + (size_t)(hd * 64 + 4 * t + (lane >> 4)) * 4096 + 2048 + (size_t)((lane >> 2) & 3) * 512 + (size_t)(28 + (lane & 3)) * 16, (u32x4){pv, pv, pv, pv});
      }
    }
  }
    STAMP(11); STAMP_OUT(0, w8);
  }
}

#define EX(x) x = __builtin_amdgcn_exp2f(x)
DI f32x16 lds_f16(unsigned a) { f32x16 v;
#pragma unroll
  for (int g = 0; g < 4; ++g) { const f32x4 c = *(const LDS_AS f32x4*)(size_t)(a + g * 1024);
#pragma unroll
    for (int e = 0; e < 4; ++e) v[4 * g + e] = c[e]; }
  return v; }
#define FSTAMP(i)
#define FFT_GAP 0
DI f32x16 FMFMA(bf16x8 a, bf16x8 b, f32x16 c) {
  f32x16 d = MFMA(a, b, c);
  if (FFT_GAP == 1) { SBAR(); __builtin_amdgcn_s_sleep(1); SBAR(); }
  return d; }
template <int SG, bool DA> DI void cmm(f32x16& re, f32x16& im, const u32x4 (&dr)[2], const u32x4 (&di)[2], const bf16x8 (&T)[6]) {
  re = (f32x16){}; im = (f32x16){};
#define CM(D, TT, ACC) ACC = DA ? FMFMA(asbf(D), TT, ACC) : FMFMA(TT, asbf(D), ACC)
#pragma unroll
  for (int s = 0; s < 2; ++s) {
    CM(dr[s], T[s], re); CM(di[s], T[s], im);
    CM(di[s], T[(SG < 0 ? 2 : 4) + s], re); CM(dr[s], T[(SG < 0 ? 4 : 2) + s], im);
  }
}
DI void pack2(u32x4 (&d)[2], const f32x16& v) { d[0] = pack8<0>(v); d[1] = pack8<1>(v); }
DI void fft_wave(int u, int lane, char* __restrict__ ws, unsigned l0 K1_STAMP_ARG) {
  const bool special = (u == 0) | (u == 224);
  const int cr = u == 224 ? 1 : 2 * u;
  const unsigned vo = lane * 16u;
  const char* ts = (const char*)&g_fft;
  { const char* xs = ws + WS_X + (size_t)(cr >> 5) * 131072 + (size_t)(cr & 31) * 16;
    const unsigned xo = (unsigned)(lane >> 1) * 4096u + (unsigned)(lane & 1) * 16u;
#pragma unroll
    for (int i = 0; i < 8; ++i) glds(l0 + i * 1024, xo, xs + i * 512);
#pragma unroll 1
    for (int i = 0; i < 5; ++i) glds4(l0 + 8192 + i * 4096, vo, ts + i * 4096); }
  unsigned L = l0 + vo, LX = l0 + (lane >> 5) * 1024 + (lane & 31) * 32; PIN(L); PIN(LX);
#define FR(i) lds_b128(L + (i) * 1024)
#define FT6(T, i) _Pragma("unroll") for (int j_ = 0; j_ < 6; ++j_) T[j_] = FR((i) + j_)
  FSTAMP(1);
  asm volatile("s_waitcnt vmcnt(0)" ::: "memory");
  __builtin_amdgcn_s_setprio(3);
  FSTAMP(2);
  { const char* wsrc = ws + WS_WT + (size_t)u * 8192;
    glds4(l0 + 52 * 1024, vo, wsrc); glds4(l0 + 56 * 1024, vo, wsrc + 4096); }
  const f32x16 twc = lds_f16(L + 20 * 1024), tws = lds_f16(L + 24 * 1024);
  bf16x8 D[6], T[6];
  FT6(D, 8); FT6(T, 14);
  u32x4 ar[2][2], ai[2][2];
  f32x16 re, im;
#pragma unroll
  for (int tau = 0; tau < 2; ++tau) {
    u32x4 zr[2], zi[2];
#pragma unroll
    for (int s = 0; s < 2; ++s) { zr[s] = __builtin_bit_cast(u32x4, lds_b128(LX + tau * 4096 + s * 2048)); zi[s] = __builtin_bit_cast(u32x4, lds_b128(LX + tau * 4096 + s * 2048 + 16)); }
    if (special) { zi[0] = (u32x4){0, 0, 0, 0}; zi[1] = zi[0]; }
    SBAR();
    if (tau == 0) cmm<-1, true>(re, im, zr, zi, D); else cmm<-1, true>(re, im, zr, zi, T);
    glds4(l0 + (28 + tau * 12) * 1024, vo, ts + (20 + tau * 12) * 1024); glds4(l0 + (32 + tau * 12) * 1024, vo, ts + (24 + tau * 12) * 1024); glds4(l0 + (36 + tau * 12) * 1024, vo, ts + (28 + tau * 12) * 1024);
    pack2(ar[tau], re * twc + im * tws); pack2(ai[tau], im * twc - re * tws);
    SBAR();
  }
  FSTAMP(3);
  asm volatile("s_waitcnt vmcnt(12)" ::: "memory");
  FSTAMP(4);
  u32x4 ur[2][2], ui[2][2];
  {
    FT6(T, 28);
    u32x4 wv[8];
#pragma unroll
    for (int g = 0; g < 8; ++g) wv[g] = __builtin_bit_cast(u32x4, FR(52 + g));
    SBAR();
    f32x16 ore, oim;
    cmm<-1, false>(re, im, ar[0], ai[0], D);
    cmm<-1, false>(ore, oim, ar[1], ai[1], T);
    f32x16 wr_, wi_;
#define UNP(B) _Pragma("unroll") for (int g = 0; g < 4; ++g) _Pragma("unroll") for (int e = 0; e < 4; ++e) { wr_[4 * g + e] = __uint_as_float(wv[B + g][e] << 16); wi_[4 * g + e] = __uint_as_float(wv[B + g][e] & 0xffff0000u); }
    UNP(4);
    const f32x16 z1r = re - ore, z1i = im - oim;
    const f32x16 u1r = z1r * wr_ - z1i * wi_, u1i = z1r * wi_ + z1i * wr_;
    UNP(0);
    const f32x16 z0r = re + ore, z0i = im + oim;
    const f32x16 u0r = z0r * wr_ - z0i * wi_, u0i = z0r * wi_ + z0i * wr_;
    pack2(ur[0], u0r + u1r); pack2(ui[0], u0i + u1i); pack2(ur[1], u0r - u1r); pack2(ui[1], u0i - u1i);
    SBAR();
  }
  FSTAMP(5);
  asm volatile("s_waitcnt vmcnt(0)" ::: "memory");
  FSTAMP(6);
  const float osc = special ? 0.5f : 1.f;
  FT6(T, 34);
#pragma unroll
  for (int kap = 0; kap < 2; ++kap) {
    bf16x8 T4[6];
    FT6(T4, 40 + kap * 6);
    SBAR();
    if (kap == 0) cmm<1, true>(re, im, ur[0], ui[0], D); else cmm<1, true>(re, im, ur[1], ui[1], T);
    u32x4 br[2], bi[2];
    pack2(br, re * twc - im * tws); pack2(bi, im * twc + re * tws);
    cmm<1, false>(re, im, br, bi, T4);
    re *= osc;
    { const unsigned ya = l0 + (lane & 31) * 128 + kap * 64 + (lane >> 5) * 32;
      *(LDS_AS u32x4*)(size_t)ya = pack8<0>(re); *(LDS_AS u32x4*)(size_t)(ya + 16) = pack8<1>(re);
      *(LDS_AS u32x4*)(size_t)(ya + 4096) = pack8<0>(im); *(LDS_AS u32x4*)(size_t)(ya + 4096 + 16) = pack8<1>(im); }
    SBAR();
  }
  { char* yd = ws + WS_Y + (size_t)cr * 4096 + vo;
    u32x4 v[8];
#pragma unroll
    for (int i = 0; i < 8; ++i) v[i] = __builtin_bit_cast(u32x4, FR(i));
#pragma unroll
    for (int i = 0; i < 4; ++i) st16<16>(yd + i * 1024, v[i]);
    if (!special) {
#pragma unroll
      for (int i = 4; i < 8; ++i) st16<16>(yd + i * 1024, v[i]); } }
}

DI f32x16 attn_slow(int h, int tile, int lane, const char* __restrict__ ws) {
  const u32x4* qp = (const u32x4*)(ws + WS_Q + (size_t)(h * 64 + tile) * 2048) + lane;
  const u32x4 q0 = qp[0], q1 = qp[64];
  const u32x4* kv = (const u32x4*)(ws + WS_KV + (size_t)h * 262144) + lane;
  float m = -INFINITY;
#pragma unroll 1
  for (int i = 0; i < 64; ++i) {
    f32x16 s = {}; s = MFMA(asbf(kv[i * 256]), asbf(q0), s); s = MFMA(asbf(kv[i * 256 + 64]), asbf(q1), s);
    m = fmaxf(m, max16(s));
  }
  m = halfmax(m);
  f32x16 o = {};
#pragma unroll 1
  for (int i = 0; i < 64; ++i) {
    f32x16 s = {}; s = MFMA(asbf(kv[i * 256]), asbf(q0), s); s = MFMA(asbf(kv[i * 256 + 64]), asbf(q1), s);
#pragma unroll
    for (int j = 0; j < 16; ++j) s[j] = __builtin_amdgcn_exp2f(s[j] - m);
    o = MFMA(asbf(kv[i * 256 + 128]), asbf(pack8<0>(s)), o); o = MFMA(asbf(kv[i * 256 + 192]), asbf(pack8<1>(s)), o);
  }
  return o;
}

#define FFT_PRIO 3
#define FFT_SLEEP 8
#define N0 13
#define N1 17
#define N2 17
#define GLD(dst, voff, sb, imm) asm volatile("global_load_dwordx4 %0, %1, %2 offset:" #imm : "=v"(dst) : "v"(voff), "s"(sb) : "memory")
#define BAR() asm volatile("s_barrier" ::: "memory")
__global__ __launch_bounds__(512, 2) void k2_kernel(const char* __restrict__ wo, char* __restrict__ ws, char* __restrict__ out) {
  __shared__ __attribute__((aligned(1024))) char lds[163840];
  const int tid = threadIdx.x, lane = tid & 63, hh = lane >> 5;
  const int w8 = __builtin_amdgcn_readfirstlane(tid >> 6), w = w8 & 3;
  const int b = blockIdx.x, xcd = b & 7, bi = b >> 3;
  const int h = 2 * xcd + (bi & 1), t = bi >> 1;
  const unsigned l0 = (unsigned)(size_t)lds;
  const unsigned vo = lane * 16u;
  STAMP_DECL; STAMP(0);
  if (w8 >= 4) {
    const int rb = 4 * xcd + bi / 7, cb = bi % 7, rt = w >> 1, ct = w & 1, r = lane & 31;
    const unsigned sw = (unsigned)(((lane & 7) ^ (4 * ((lane >> 4) & 1))) * 16);
    const int u = 32 * xcd + bi;
    __builtin_amdgcn_s_waitcnt(0x0F70);
    if (w == 0) {
      __builtin_amdgcn_s_sleep(FFT_SLEEP);
      if (u < 225) fft_wave(u, lane, ws, l0 + 102400 K1_STAMP_PASS);
      asm volatile("s_waitcnt vmcnt(0)" ::: "memory");
      if (lane == 0) set_flag(ws + WS_FLG + u * 4);
    } else {
      __builtin_amdgcn_s_sleep(100);
      if (bi < 28) { const unsigned so = (unsigned)(lane >> 3) * 896u + sw; const char* sb = wo + cb * 128;
#pragma unroll 1
        for (int j = w - 1; j < 56; j += 3) { glds(l0 + j * 1024, so, sb + (size_t)j * 8 * 896); __builtin_amdgcn_s_sleep(2); } }
    }
    STAMP(1);
    if (bi >= 28) { STAMP(11); STAMP_OUT(1, w8); ENDPGM(); }
    u32x4 f[28];
    float g;
    { const char* db = (const char*)&g_gtab + (size_t)(2 * cb + ct) * 28672;
#define GLD4(I) do { const char* d0 = db + (I) * 1024; GLD(f[I], vo, d0, 0); GLD(f[I + 1], vo, d0, 1024); GLD(f[I + 2], vo, d0, 2048); GLD(f[I + 3], vo, d0, 3072); __builtin_amdgcn_s_sleep(2); } while (0)
      GLD4(0); GLD4(4); GLD4(8); GLD4(12); GLD4(16); GLD4(20); GLD4(24);
      asm volatile("global_load_dword %0, %1, %2" : "=v"(g) : "v"(r * 4u), "s"(ws + WS_G + (size_t)(rb * 64 + rt * 32) * 4) : "memory"); }
    __builtin_amdgcn_s_setprio(2);
#define F8(I) "+v"(f[I]), "+v"(f[I + 1]), "+v"(f[I + 2]), "+v"(f[I + 3]), "+v"(f[I + 4]), "+v"(f[I + 5]), "+v"(f[I + 6])
    STAMP(2);
    BAR();
#define YREADY(OK) do { u32x4 v_; asm volatile("global_load_dwordx4 %0, %1, %2 sc1\n\ts_waitcnt vmcnt(0)" : "=&v"(v_) : "v"(vo), "s"(ws + WS_FLG) : "memory"); \
      OK = __all((v_[0] & v_[1] & v_[2] & v_[3]) != 0u); } while (0)
    const unsigned yso = (unsigned)(lane >> 3) * 4096u + sw + (unsigned)w * (14u * 8u * 4096u); const char* ysb = ws + WS_Y + rb * 128;
    bool passed_b2 = false;
#pragma unroll 1
    for (int spin = 0; spin < (1 << 20); ++spin) {
      bool ok; YREADY(ok);
      if (ok) break;
      if (!passed_b2 && spin >= 1) { BAR(); passed_b2 = true; }
      __builtin_amdgcn_s_sleep(10);
    }
    STAMP(3);
#pragma unroll
    for (int j = 0; j < 14; ++j) glds_dev(l0 + 57344 + (w * 14 + j) * 1024, yso + j * 32768u, ysb);
    __builtin_amdgcn_s_setprio(0);
    if (!passed_b2) BAR();
    asm volatile("s_waitcnt vmcnt(0)" : F8(0), F8(7), F8(14), F8(21), "+v"(g)::"memory");
    STAMP(4);
    BAR();
    const char* ob_ = ws + WS_O + (size_t)(2 * rb + rt) * 32768 + ct * 16384;
#define OLD(J, IMM) asm volatile("global_load_dwordx4 %0, %1, %2 offset:" #IMM " sc1" : "+v"(fo[J]) : "v"(vo), "s"(og) : "memory")
#define OLD4(GQ) do { const char* og = ob_ + (GQ) * 4096; OLD(4 * (GQ), 0); OLD(4 * (GQ) + 1, 1024); OLD(4 * (GQ) + 2, 2048); OLD(4 * (GQ) + 3, 3072); } while (0)
    STAMP(5);
    const int q = (lane & 15) >> 2, p = lane & 3, G = (lane >> 4) & 1;
    f32x16 acca = {}, accb = {};
    { unsigned ya = l0 + 57344 + (8 * hh + q) * 128 + (((q >> 1) ^ rt) * 64) + G * 32 + p * 8; PIN(ya);
      bf16x8 y[4];
#pragma unroll
      for (int i = 0; i < 4; ++i) y[i] = lds_tr2(ya + i * 2048, ya + i * 2048 + 512);
#pragma unroll
      for (int i = 0; i < 28; ++i) { SBAR(); acca = MFMA(asbf(f[i]), y[i & 3], acca); SBAR(); if (i + 4 < 28) y[i & 3] = lds_tr2(ya + (i + 4) * 2048, ya + (i + 4) * 2048 + 512); } }
    SBAR();
    STAMP(6);
    u32x4 fo[16];
#pragma unroll
    for (int e = 0; e < 16; ++e) asm volatile("" : "=v"(fo[e]));
    OLD4(0); OLD4(1); OLD4(2); OLD4(3);
    unsigned w0 = l0 + ct * 28672 + (4 * hh + q) * 128 + (((q >> 1) ^ ct) * 64) + (4 * (p & 1) + 2 * G + (p >> 1)) * 8, w2 = w0 - hh * 512, w1 = w0 ^ 64, w3 = w2 ^ 64;
    PIN(w0); PIN(w1); PIN(w2); PIN(w3);
#define WOFF(J) (((J) >> 1) * 3584 + ((J) & 1) * 2048)
#define WOF(M, J) lds_tr2(((M) ? w1 : w0) + WOFF(J), (((J) & 1) ? ((M) ? w3 : w2) : ((M) ? w1 : w0)) + WOFF(J) + 1024)
    bf16x8 wn[2][8];
#pragma unroll
    for (int e = 0; e < 8; ++e) { wn[0][e] = WOF(0, e); wn[1][e] = WOF(1, e); }
    acca *= g * (2.0f / (2048.f * 448.f));
    const float g1 = rbf(1.f - g);
    f32x16 accs = {};
    BAR();
#define MX4(Y) mx = max(max(mx, Y[0]), Y[1]), mx = max(max(mx, Y[2]), Y[3])
#define F4(I) "+v"(fo[I]), "+v"(fo[I + 1]), "+v"(fo[I + 2]), "+v"(fo[I + 3])
#define OGRP(GQ, N, RELOAD)                                                                                                    \
    asm volatile("s_waitcnt vmcnt(" #N ")" : F4(4 * GQ)::"memory");                                                            \
    _Pragma("unroll 1") for (int spin = 0; spin < (1 << 20); ++spin) {                                                         \
      unsigned mx = 0; MX4(fo[4 * GQ]); MX4(fo[4 * GQ + 1]); MX4(fo[4 * GQ + 2]); MX4(fo[4 * GQ + 3]);                             \
      if (__builtin_expect(!__any(mx == ~0u), 1)) break;                                                                       \
      asm volatile("s_waitcnt vmcnt(0)" ::: "memory");                                                                         \
      RELOAD;                                                                                                                  \
      asm volatile("s_waitcnt vmcnt(" #N ")" : F4(4 * GQ)::"memory");                                                          \
    }                                                                                                                          \
    _Pragma("unroll") for (int e = 0; e < 4; ++e) { const int k_ = (GQ & 1) * 4 + e; SBAR();                                   \
      accs = MFMA(wn[1][k_], asbf(fo[4 * GQ + e]), accs); accb = MFMA(wn[0][k_], asbf(fo[4 * GQ + e]), accb); SBAR();           \
      if (GQ < 2) { wn[0][k_] = WOF(0, 4 * GQ + 8 + e); wn[1][k_] = WOF(1, 4 * GQ + 8 + e); } }
    OGRP(0, 12, OLD4(0); OLD4(1); OLD4(2); OLD4(3))
    STAMP(7);
    OGRP(1, 8, OLD4(1); OLD4(2); OLD4(3))
    OGRP(2, 4, OLD4(2); OLD4(3))
    OGRP(3, 0, OLD4(3))
    STAMP(8);
    f32x4* ex = (f32x4*)(lds + 57344) + lane;
#pragma unroll
    for (int e = 0; e < 4; ++e) ex[(w ^ 1) * 256 + e * 64] = (f32x4){accs[4 * e], accs[4 * e + 1], accs[4 * e + 2], accs[4 * e + 3]};
    f32x16 res = accb * g1 + acca;
    __builtin_amdgcn_s_waitcnt(0xC07F);
    BAR();
    STAMP(10);
#pragma unroll
    for (int e = 0; e < 4; ++e) { const f32x4 v = ex[w * 256 + e * 64];
#pragma unroll
      for (int k_ = 0; k_ < 4; ++k_) res[4 * e + k_] = fmaf(v[k_], g1, res[4 * e + k_]); }
    u32x4* od_ = (u32x4*)(out + (size_t)(64 * r + 2 * rb + rt) * 896 + (size_t)(cb * 64 + ct * 32 + 16 * hh) * 2);
    st16<AUX3>(od_, pack8<0>(res)); st16<AUX3>(od_ + 1, pack8<1>(res));
    STAMP(11); STAMP_OUT(1, w8);
    ENDPGM();
  }
  const int t0 = w == 0 ? 0 : w == 1 ? N0 : w == 2 ? N0 + N1 : N0 + N1 + N2;
  const int n = w == 0 ? N0 : w == 1 ? N1 : w == 2 ? N2 : 64 - N0 - N1 - N2;
  unsigned ko = ((t0 + t) & 63) * 4096u;
#define KNEXT() ko = (ko + 4096u) & 0x3FFFFu
  const auto kvr = __builtin_amdgcn_make_buffer_rsrc((void*)(ws + WS_KV + (size_t)h * 262144), 0, 262144, 0x00020000);
#define KV(off, idx) __builtin_bit_cast(u32x4, __builtin_amdgcn_raw_buffer_load_b128(kvr, vo + (idx) * 1024, (off), 0))
#define KV4(K0, K1, V0, V1) do { K0 = KV(ko, 0); K1 = KV(ko, 1); V0 = KV(ko, 2); V1 = KV(ko, 3); KNEXT(); } while (0)
  const u32x4* qp = (const u32x4*)(ws + WS_Q + (size_t)(h * 64 + 4 * t) * 2048) + lane;
  const u32x4 kr0 = KV(4 * t * 4096, 0), kr1 = KV(4 * t * 4096, 1);
  const int ia = ((w + 1) & 3) * 128, ib = ((w + 2) & 3) * 128, ic = ((w + 3) & 3) * 128, id_ = w * 128;
  u32x4 qa0 = qp[ia], qa1 = qp[ia + 64], qb0 = qp[ib], qb1 = qp[ib + 64], qc0 = qp[ic], qc1 = qp[ic + 64], qd0 = qp[id_], qd1 = qp[id_ + 64];
  SBAR();
  u32x4 kA0, kA1, vA0, vA1, kB0, kB1, vB0, vB1, kC0, kC1, vC0, vC1, kD0, kD1, vD0, vD1 = {0, 0, 0, 0};
  KV4(kA0, kA1, vA0, vA1); SBAR();
  KV4(kB0, kB1, vB0, vB1); SBAR();
  KV4(kC0, kC1, vC0, vC1); SBAR();
  f32x16 oa = {}, ob = {}, oc = {}, od = {}, sa, sb;
  const f32x16 zero = {};
  u32x4 p0, p1 = {0, 0, 0, 0};
  PIN(oa); PIN(ob); PIN(oc); PIN(od); PIN(p1); PIN(vD1); SBAR();
  {
    const bool selA = (h & 1) && hh == 0, selB = !(h & 1) && hh == 1;
    f32x16 s0 = MFMA(asbf(kr0), asbf(qa0), zero); s0 = MFMA(asbf(kr1), asbf(qa1), s0);
    f32x16 s1 = MFMA(asbf(kr0), asbf(qb0), zero); s1 = MFMA(asbf(kr1), asbf(qb1), s1);
    f32x16 s2 = MFMA(asbf(kr0), asbf(qc0), zero); s2 = MFMA(asbf(kr1), asbf(qc1), s2);
    f32x16 s3 = MFMA(asbf(kr0), asbf(qd0), zero); s3 = MFMA(asbf(kr1), asbf(qd1), s3);
#define REF(S, Q0, Q1) do { const unsigned nm = pk(-max16(S), 0.f); Q0[0] = selA ? nm : Q0[0]; Q1[2] = selB ? nm : Q1[2]; } while (0)
    REF(s0, qa0, qa1);
    sa = MFMA(asbf(kA0), asbf(qa0), zero); sa = MFMA(asbf(kA1), asbf(qa1), sa);
    REF(s1, qb0, qb1); REF(s2, qc0, qc1); REF(s3, qd0, qd1);
  }
  sb = zero;
  PIN(sa); STAMP(1); CSTAMP(8);
#define SLOT(S, N, KN0, KN1, QN0, QN1, OP, VP, OC, VC, LD1, LD2)                                                              \
  do {                                                                                                                        \
    SBAR();                                                                                                                   \
    EX(S[0]); EX(S[1]); p1[2] = pk(N[12], N[13]); EX(S[2]); EX(S[3]); p1[3] = pk(N[14], N[15]); PIN(S); PIN(p1); SBAR();      \
    N = MFMA(asbf(KN0), asbf(QN0), zero); PIN(N); SBAR();                                                                             \
    EX(S[4]); EX(S[5]); p0[0] = pk(S[0], S[1]); EX(S[6]); EX(S[7]); p0[1] = pk(S[2], S[3]); PIN(S); PIN(p0); SBAR();          \
    N = MFMA(asbf(KN1), asbf(QN1), N); PIN(N); SBAR();                                                                                \
    LD1;                                                                                                                      \
    EX(S[8]); EX(S[9]); p0[2] = pk(S[4], S[5]); EX(S[10]); EX(S[11]); p0[3] = pk(S[6], S[7]); PIN(S); PIN(p0); SBAR();        \
    OP = MFMA(asbf(VP), asbf(p1), OP); PIN(OP); SBAR();                                                                                \
    EX(S[12]); EX(S[13]); p1[0] = pk(S[8], S[9]); EX(S[14]); EX(S[15]); p1[1] = pk(S[10], S[11]); PIN(S); PIN(p1); SBAR();    \
    OC = MFMA(asbf(VC), asbf(p0), OC); PIN(OC); SBAR();                                                                                \
    LD2;                                                                                                                      \
  } while (0)
#define ITS(k)
  unsigned ca = l0 + 114688 + vo + ((w + 1) & 3) * 12288, cb_ = l0 + 114688 + vo + ((w + 2) & 3) * 12288 + 4096, cc = l0 + 114688 + vo + ((w + 3) & 3) * 12288 + 8192, cr_ = l0 + 114688 + vo + w * 12288;
  PIN(ca); PIN(cb_); PIN(cc); PIN(cr_);
#define TILE(X, P, N, MID)                                                                                                    \
    SLOT(sa, sb, k##X##0, k##X##1, qb0, qb1, od, v##P##1, oa, v##X##0, (void)0, KV4(k##P##0, k##P##1, v##P##0, v##P##1));     \
    MID;                                                                                                                      \
    SLOT(sb, sa, k##X##0, k##X##1, qc0, qc1, oa, v##X##1, ob, v##X##0, (void)0, (void)0);                                     \
    SLOT(sa, sb, k##X##0, k##X##1, qd0, qd1, ob, v##X##1, oc, v##X##0, (void)0, __builtin_amdgcn_s_waitcnt(0x0F78));          \
    SLOT(sb, sa, k##N##0, k##N##1, qa0, qa1, oc, v##X##1, od, v##X##0, (void)0, (void)0);
  __builtin_amdgcn_s_waitcnt(0x0F78);
  int i = n - 1;
  do {
    ITS(n - 1 - i);
    TILE(A, D, B, (void)0)
    ITS(n - i);
    TILE(B, A, C, (void)0)
    ITS(n + 1 - i);
    TILE(C, B, D, if (i == 4) BAR())
    ITS(n + 2 - i);
    TILE(D, C, A, (void)0)
    i -= 4;
  } while (i > 0);
  ITS(n - 1);
  STAMP(4);
#define PUT2(O, A, I) do { *(LDS_AS f32x4*)(size_t)(A + (I) * 1024) = (f32x4){O[4 * (I)], O[4 * (I) + 1], O[4 * (I) + 2], O[4 * (I) + 3]}; \
                           *(LDS_AS f32x4*)(size_t)(A + (I) * 1024 + 1024) = (f32x4){O[4 * (I) + 4], O[4 * (I) + 5], O[4 * (I) + 6], O[4 * (I) + 7]}; } while (0)
  SLOT(sa, sb, kA0, kA1, qb0, qb1, od, vD1, oa, vA0, (void)0, (void)0);
  SLOT(sb, sa, kA0, kA1, qc0, qc1, oa, vA1, ob, vA0, (void)0, (void)0);
  SLOT(sa, sb, kA0, kA1, qd0, qd1, ob, vA1, oc, vA0, PUT2(oa, ca, 0), PUT2(oa, ca, 2));
  SBAR();
  EX(sb[0]); EX(sb[1]); p1[2] = pk(sa[12], sa[13]); EX(sb[2]); EX(sb[3]); p1[3] = pk(sa[14], sa[15]); PIN(sb); PIN(p1); SBAR();
  oc = MFMA(asbf(vA1), asbf(p1), oc); PIN(oc); SBAR();
  EX(sb[4]); EX(sb[5]); p0[0] = pk(sb[0], sb[1]); EX(sb[6]); EX(sb[7]); p0[1] = pk(sb[2], sb[3]); PIN(sb); PIN(p0); SBAR();
  PUT2(ob, cb_, 0); SBAR();
  EX(sb[8]); EX(sb[9]); p0[2] = pk(sb[4], sb[5]); EX(sb[10]); EX(sb[11]); p0[3] = pk(sb[6], sb[7]); PIN(sb); PIN(p0); SBAR();
  PUT2(ob, cb_, 2); SBAR();
  od = MFMA(asbf(vA0), asbf(p0), od); PIN(od); SBAR();
  EX(sb[12]); EX(sb[13]); p1[0] = pk(sb[8], sb[9]); EX(sb[14]); EX(sb[15]); p1[1] = pk(sb[10], sb[11]); PIN(sb); PIN(p1); SBAR();
  PUT2(oc, cc, 0); SBAR();
  p1[2] = pk(sb[12], sb[13]); p1[3] = pk(sb[14], sb[15]);
  PUT2(oc, cc, 2); SBAR();
  od = MFMA(asbf(vA1), asbf(p1), od);
  PIN(od); STAMP(2); CSTAMP(9);
  f32x16 o = od;
  __builtin_amdgcn_s_waitcnt(0xC07F);
  BAR();
  STAMP(3);
#pragma unroll
  for (int j = 0; j < 12; ++j) { const f32x4 v = *(const LDS_AS f32x4*)(size_t)(cr_ + j * 1024); const int e = 4 * (j & 3); o[e] += v[0]; o[e + 1] += v[1]; o[e + 2] += v[2]; o[e + 3] += v[3]; }
  float l;
  { auto rr = __builtin_amdgcn_permlane32_swap(__float_as_uint(o[12]), __float_as_uint(o[12]), false, false); l = __uint_as_float(rr[1]); }
  f32x2 sm = (f32x2){o[0], o[1]};
#pragma unroll
  for (int j = 2; j < 16; j += 2) sm += (f32x2){o[j], o[j + 1]};
  const bool ok = (l >= 1.0e-12f) && (l <= 1.0e12f) && (fabsf(sm[0] + sm[1]) <= 3.0e38f);
  if (__builtin_expect(!__all(ok), 0)) {
    o = attn_slow(h, 4 * t + w, lane, ws);
    auto rr = __builtin_amdgcn_permlane32_swap(__float_as_uint(o[12]), __float_as_uint(o[12]), false, false); l = __uint_as_float(rr[1]);
  }
  o *= __builtin_amdgcn_rcpf(l);
  { u32x4 o0 = pack8<0>(o), o1 = pack8<1>(o);
    if (hh == 1) { o1[2] = 0; o1[3] = 0; }
    u32x4* odst = (u32x4*)(ws + WS_O + (size_t)((4 * t + w) * 16 + h) * 2048) + lane;
#pragma unroll
    for (int i = 0; i < 4; ++i) { o0[i] = min(o0[i], 0xFFFFFFFEu); o1[i] = min(o1[i], 0xFFFFFFFEu); }
    st16<16>(odst, o0); st16<16>(odst + 64, o1); }
  STAMP(11);
  BAR();
  STAMP_OUT(1, w);
}

extern "C" void kernel_launch(void* const* d_in, const int* in_sizes, int n_in, void* d_out, int out_size, void* d_ws, size_t ws_size, hipStream_t stream) {
  char* ws = (char*)d_ws;
  k1_kernel<<<256, 512, 0, stream>>>((const char*)d_in[0], (const char*)d_in[4], (const char*)d_in[6], (const char*)d_in[1], (const char*)d_in[2], (const char*)d_in[5], ws);
  k2_kernel<<<256, 512, 0, stream>>>((const char*)d_in[5], ws, (char*)d_out);
}
```

```cpp
#include <hip/hip_runtime.h>
#include <stdint.h>

typedef short bf16x8 __attribute__((ext_vector_type(8)));
typedef short s16x4 __attribute__((ext_vector_type(4)));
typedef float f32x16 __attribute__((ext_vector_type(16)));
typedef float f32x4 __attribute__((ext_vector_type(4)));
typedef float f32x2 __attribute__((ext_vector_type(2)));
typedef unsigned u32x4 __attribute__((ext_vector_type(4)));
typedef __bf16 bf16x2_t __attribute__((ext_vector_type(2)));
#define DI __device__ __forceinline__
#define LDS_AS __attribute__((address_space(3)))

namespace tab {
constexpr double PI = 3.14159265358979323846264338327950288;
constexpr double tsin(double x) { double x2 = x * x, t = x, s = x; for (int i = 1; i <= 10; ++i) { t *= -x2 / ((2 * i) * (2 * i + 1)); s += t; } return s; }
constexpr double tcos(double x) { double x2 = x * x, t = 1, s = 1; for (int i = 1; i <= 10; ++i) { t *= -x2 / ((2 * i - 1) * (2 * i)); s += t; } return s; }
constexpr double cosr(int m, int N) {
  m %= N; if (m < 0) m += N; if (2 * m > N) m = N - m;
  bool neg = false; if (4 * m > N) { m = N / 2 - m; neg = true; }
  double r = (8 * m > N) ? tsin(2 * PI * (N / 4 - m) / N) : tcos(2 * PI * m / N); return neg ? -r : r; }
constexpr double sinr(int m, int N) { return cosr(m - N / 4, N); }
constexpr uint32_t f2bf(double d) { float f = (float)d; uint32_t u = __builtin_bit_cast(uint32_t, f); u += 0x7fffu + ((u >> 16) & 1u); return u >> 16; }
constexpr int crow(int r, int hh) { return (r & 3) + 8 * (r >> 2) + 4 * hh; }
constexpr int krow(int s, int hh, int j) { return 16 * s + 8 * (j >> 2) + 4 * hh + (j & 3); }
constexpr int sigma(int r) { return 16 * ((r >> 2) & 1) + 4 * (r >> 3) + (r & 3); }
struct Base448 { uint16_t c[448], ns[448]; };
constexpr Base448 mk448() { Base448 b{}; for (int i = 0; i < 448; ++i) { b.c[i] = f2bf(cosr(i, 448)); b.ns[i] = f2bf(-sinr(i, 448)); } return b; }
constexpr Base448 B448 = mk448();
constexpr uint32_t Fval(int j, int c) { return c == 1 ? B448.c[(j * 224) % 448] : ((c & 1) ? B448.ns[(j * (c >> 1)) % 448] : B448.c[(j * (c >> 1)) % 448]); }
struct Tile { uint32_t v[28 * 64 * 4]; };
constexpr Tile mkF(int jt) { Tile t{}; for (int j = 0; j < 32; ++j) for (int c = 0; c < 224; ++c) t.v[j * 224 + c] = Fval(jt * 32 + j, 2 * c) | (Fval(jt * 32 + j, 2 * c + 1) << 16); return t; }
constexpr Tile mkG(int jt) { Tile t{}; for (int ks = 0; ks < 28; ++ks) for (int l = 0; l < 64; ++l) { int j = jt * 32 + sigma(l & 31), c0 = ks * 16 + 8 * (l >> 5);
  for (int e = 0; e < 4; ++e) t.v[(ks * 64 + l) * 4 + e] = Fval(j, c0 + 2 * e) | (Fval(j, c0 + 2 * e + 1) << 16); } return t; }
template <int T> constexpr Tile FT = mkF(T);
template <int T> constexpr Tile GT = mkG(T);
struct Tab14 { Tile t[14]; };
struct Fft { uint32_t ta[12][256]; float tw[8][256]; uint32_t tb[24][256]; };
struct Base2k { uint16_t c[2048]; };
constexpr Base2k mk2k() { Base2k b{}; for (int i = 0; i < 2048; ++i) b.c[i] = f2bf(cosr(i, 2048)); return b; }
constexpr Base2k B2K = mk2k();
constexpr uint32_t cs2k(int m, int which) { return B2K.c[(m + (which == 0 ? 0 : which == 1 ? 1536 : 512)) & 2047]; }
constexpr int fang(int set, int r, int k) {
  return set == 0 ? 64 * k * r : set == 1 ? (64 * k + 1) * r : set == 2 ? 32 * (2 * k + 1) * r : set == 3 ? 32 * (2 * r + 1) * k : set == 4 ? 64 * sigma(r) * k : (64 * sigma(r) + 1) * k; }
constexpr Fft mkFft() { Fft f{};
  for (int l = 0; l < 64; ++l) { const int r = l & 31, hh = l >> 5;
    for (int e = 0; e < 4; ++e) {
      for (int set = 0; set < 6; ++set) for (int w = 0; w < 3; ++w) for (int s = 0; s < 2; ++s) {
        const uint32_t v = cs2k(fang(set, r, krow(s, hh, 2 * e)), w) | (cs2k(fang(set, r, krow(s, hh, 2 * e + 1)), w) << 16);
        if (set < 2) f.ta[(set * 3 + w) * 2 + s][l * 4 + e] = v; else f.tb[((set - 2) * 3 + w) * 2 + s][l * 4 + e] = v; }
      for (int g = 0; g < 4; ++g) { const int m = crow(4 * g + e, hh) * r; f.tw[g][l * 4 + e] = (float)cosr(m, 1024); f.tw[4 + g][l * 4 + e] = (float)sinr(m, 1024); }
    } }
  return f; }
constexpr Fft FFT = mkFft();
}
#define TAB14(X) {{tab::X<0>, tab::X<1>, tab::X<2>, tab::X<3>, tab::X<4>, tab::X<5>, tab::X<6>, tab::X<7>, tab::X<8>, tab::X<9>, tab::X<10>, tab::X<11>, tab::X<12>, tab::X<13>}}
__device__ __attribute__((aligned(256))) const tab::Tab14 g_ftab = TAB14(FT);
__device__ __attribute__((aligned(256))) const tab::Tab14 g_gtab = TAB14(GT);
__device__ __attribute__((aligned(256))) const tab::Fft g_fft = tab::FFT;

#define P_SLEEP 2
#define FFT_SLEEP 20
#define H_SLEEP 60
#define CODE_LINES 238
#define P_REL
#define P_AT 215
#define TAGINC 0x9E3779B97F4A7C15ull
constexpr unsigned WS_Q = 0;
constexpr unsigned WS_KV = WS_Q + (2u << 20);
constexpr unsigned WS_X = WS_KV + (4u << 20);
constexpr unsigned WS_WT = WS_X + (2u << 20);
constexpr unsigned WS_G = WS_WT + (2u << 20);
constexpr unsigned WS_EP = WS_G + 16384;
constexpr unsigned WS_YF = WS_G + 32768;
constexpr unsigned WS_PF = WS_G + 65536;
constexpr unsigned WS_O = WS_G + (1u << 20);
constexpr unsigned WS_Y = WS_O + (2u << 20);

#define STAMP_DECL
#define STAMP(i)
#define STAMP_OUT()
#define MFMA(a, b, c) __builtin_amdgcn_mfma_f32_32x32x16_bf16((a), (b), (c), 0, 0, 0)
#define SBAR() __builtin_amdgcn_sched_barrier(0)
#define PIN(x) asm volatile("" : "+v"(x))
#define BAR() asm volatile("s_barrier" ::: "memory")
#define ENDPGM() __builtin_amdgcn_endpgm()
DI unsigned pk(float lo, float hi) { f32x2 v = {lo, hi}; return __builtin_bit_cast(unsigned, __builtin_convertvector(v, bf16x2_t)); }
DI bf16x8 asbf(u32x4 v) { return __builtin_bit_cast(bf16x8, v); }
template <int S> DI u32x4 pack8(const f32x16& a) { return (u32x4){pk(a[8 * S], a[8 * S + 1]), pk(a[8 * S + 2], a[8 * S + 3]), pk(a[8 * S + 4], a[8 * S + 5]), pk(a[8 * S + 6], a[8 * S + 7])}; }
DI float rbf(float v) { return __uint_as_float(pk(v, v) << 16); }
DI void glds(unsigned lds, unsigned voff, const void* sbase) {
  asm volatile("s_mov_b32 m0, %0\n\ts_nop 0\n\tglobal_load_lds_dwordx4 %1, %2" ::"s"(lds), "v"(voff), "s"(sbase) : "memory"); }
DI void glds4(unsigned lds, unsigned voff, const void* sbase) {
  asm volatile("s_mov_b32 m0, %0\n\ts_nop 0\n\tglobal_load_lds_dwordx4 %1, %2\n\tglobal_load_lds_dwordx4 %1, %2 offset:1024\n\t"
               "global_load_lds_dwordx4 %1, %2 offset:2048\n\tglobal_load_lds_dwordx4 %1, %2 offset:3072" ::"s"(lds), "v"(voff), "s"(sbase) : "memory"); }
DI void glds_dev(unsigned lds, unsigned voff, const void* sbase) {
  asm volatile("s_mov_b32 m0, %0\n\ts_nop 0\n\tglobal_load_lds_dwordx4 %1, %2 sc1" ::"s"(lds), "v"(voff), "s"(sbase) : "memory"); }
DI void glds4_dev(unsigned lds, unsigned voff, const void* sbase) {
  asm volatile("s_mov_b32 m0, %0\n\ts_nop 0\n\tglobal_load_lds_dwordx4 %1, %2 sc1\n\tglobal_load_lds_dwordx4 %1, %2 offset:1024 sc1\n\t"
               "global_load_lds_dwordx4 %1, %2 offset:2048 sc1\n\tglobal_load_lds_dwordx4 %1, %2 offset:3072 sc1" ::"s"(lds), "v"(voff), "s"(sbase) : "memory"); }
DI void touch(unsigned lds, unsigned voff, const void* sbase) {
  asm volatile("s_mov_b32 m0, %0\n\ts_nop 0\n\tglobal_load_lds_dword %1, %2" ::"s"(lds), "v"(voff), "s"(sbase) : "memory"); }
DI void st16(unsigned voff, const void* sbase, u32x4 v) {
  asm volatile("global_store_dwordx4 %0, %1, %2 sc1\n\ts_nop 1" ::"v"(voff), "v"(v), "s"(sbase) : "memory"); }
typedef __attribute__((address_space(1))) unsigned gu32;
typedef __attribute__((address_space(1))) unsigned long long gu64;
typedef unsigned u32x2 __attribute__((ext_vector_type(2)));
typedef short v4i16_t __attribute__((ext_vector_type(4)));
DI bf16x8 lds_b128(unsigned a) { return *(const LDS_AS bf16x8*)(size_t)a; }
DI bf16x8 lds_tr2(unsigned a0, unsigned a1) {
  v4i16_t lo = __builtin_amdgcn_ds_read_tr16_b64_v4i16((LDS_AS v4i16_t*)(size_t)a0), hi = __builtin_amdgcn_ds_read_tr16_b64_v4i16((LDS_AS v4i16_t*)(size_t)a1);
  return __builtin_shufflevector(lo, hi, 0, 1, 2, 3, 4, 5, 6, 7); }
DI float halfmax(float m) { auto r = __builtin_amdgcn_permlane32_swap(__float_as_uint(m), __float_as_uint(m), false, false); return fmaxf(__uint_as_float(r[0]), __uint_as_float(r[1])); }
DI float max16(const f32x16& s) { float m = fmaxf(s[0], s[1]);
#pragma unroll
  for (int i = 2; i < 16; i += 2) m = fmaxf(fmaxf(m, s[i]), s[i + 1]);
  return m; }
DI void wait_p1(const char* pf, unsigned i0, unsigned i1, unsigned tlo, unsigned thi) {
#pragma unroll 1
  for (int spin = 0; spin < (1 << 16); ++spin) {
    u32x2 a, b;
    asm volatile("global_load_dwordx2 %0, %2, %4 sc1\n\tglobal_load_dwordx2 %1, %3, %4 sc1\n\ts_waitcnt vmcnt(0)" : "=&v"(a), "=&v"(b) : "v"(i0 * 128u), "v"(i1 * 128u), "s"(pf) : "memory");
    if (__all(((a[0] ^ tlo) | (a[1] ^ thi) | (b[0] ^ tlo) | (b[1] ^ thi)) == 0u)) break;
    __builtin_amdgcn_s_sleep(P_SLEEP);
  }
}

constexpr int K1_SLOT = 32768;
struct K1Frag { bf16x8 x0, x1, w0, w1; };
template <int TR> DI void k1_compute(unsigned xa, unsigned wa, f32x16 (&acc)[2][2], const char* ws, const unsigned (&po)[4], unsigned po2) {
  unsigned ax[4], aw0 = wa, aw1 = wa ^ 64;
#pragma unroll
  for (int ks = 0; ks < 4; ++ks) ax[ks] = xa ^ (ks * 32);
  K1Frag fa, fb = {};
#define K1_M(I, J, G) acc[I][J] = TR ? MFMA(G.w##J, G.x##I, acc[I][J]) : MFMA(G.x##I, G.w##J, acc[I][J])
#define K1_RD(F, KS, G) do {                                                                                                      \
    F.x0 = lds_b128(ax[KS]); SBAR(); K1_M(0, 0, G); SBAR();                                                                       \
    F.w0 = lds_tr2(aw0 + KS * 2048, aw0 + KS * 2048 + 512); if (KS == 3) { aw0 += d; PIN(aw0); } SBAR(); K1_M(1, 0, G); SBAR();   \
    F.x1 = lds_b128(ax[KS] + 4096); ax[KS] += d; PIN(ax[KS]); SBAR(); K1_M(0, 1, G); SBAR();                                      \
    F.w1 = lds_tr2(aw1 + KS * 2048, aw1 + KS * 2048 + 512); if (KS == 3) { aw1 += d; PIN(aw1); } SBAR(); K1_M(1, 1, G); SBAR(); } while (0)
  __builtin_amdgcn_s_waitcnt(0xC07F);
#pragma unroll 1
  for (int i = 0; i < 7; ++i) {
    BAR();
    const unsigned d = i == 4 ? (unsigned)(-4 * K1_SLOT) : (unsigned)K1_SLOT;
    K1_RD(fa, 0, fb); K1_RD(fb, 1, fa);
    K1_RD(fa, 2, fb); K1_RD(fb, 3, fa);
  }
  K1_M(0, 0, fb); K1_M(1, 0, fb); K1_M(0, 1, fb); K1_M(1, 1, fb);
}

#define EX(x) x = __builtin_amdgcn_exp2f(x)
DI f32x16 lds_f16(unsigned a) { f32x16 v;
#pragma unroll
  for (int g = 0; g < 4; ++g) { const f32x4 c = *(const LDS_AS f32x4*)(size_t)(a + g * 1024);
#pragma unroll
    for (int e = 0; e < 4; ++e) v[4 * g + e] = c[e]; }
  return v; }
template <int SG, bool DA> DI void cmm(f32x16& re, f32x16& im, const u32x4 (&dr)[2], const u32x4 (&di)[2], const bf16x8 (&T)[6]) {
  re = (f32x16){}; im = (f32x16){};
#define CM(D, TT, ACC) ACC = DA ? MFMA(asbf(D), TT, ACC) : MFMA(TT, asbf(D), ACC)
#pragma unroll
  for (int s = 0; s < 2; ++s) {
    CM(dr[s], T[s], re); CM(di[s], T[s], im);
    CM(di[s], T[(SG < 0 ? 2 : 4) + s], re); CM(dr[s], T[(SG < 0 ? 4 : 2) + s], im);
  }
}
DI void pack2(u32x4 (&d)[2], const f32x16& v) { d[0] = pack8<0>(v); d[1] = pack8<1>(v); }
DI void fft_wave(int u, int lane, char* __restrict__ ws, unsigned l0) {
  const bool special = (u == 0) | (u == 224);
  const int cr = u == 224 ? 1 : 2 * u;
  const unsigned vo = lane * 16u;
  { const char* xs = ws + WS_X + (size_t)(cr >> 5) * 131072 + (size_t)(cr & 31) * 16;
    const unsigned xo = (unsigned)(lane >> 1) * 4096u + (unsigned)(lane & 1) * 16u;
#pragma unroll
    for (int i = 0; i < 8; ++i) glds_dev(l0 + i * 1024, xo, xs + i * 512);
    const char* wsrc = ws + WS_WT + (size_t)u * 8192;
    glds4_dev(l0 + 52 * 1024, vo, wsrc); glds4_dev(l0 + 56 * 1024, vo, wsrc + 4096); }
  unsigned L = l0 + vo, LX = l0 + (lane >> 5) * 1024 + (lane & 31) * 32; PIN(L); PIN(LX);
#define FR(i) lds_b128(L + (i) * 1024)
#define FT6(T, i) _Pragma("unroll") for (int j_ = 0; j_ < 6; ++j_) T[j_] = FR((i) + j_)
  const f32x16 twc = lds_f16(L + 20 * 1024), tws = lds_f16(L + 24 * 1024);
  bf16x8 D[6], T[6];
  FT6(D, 8); FT6(T, 14);
  asm volatile("s_waitcnt vmcnt(8)" ::: "memory");
  __builtin_amdgcn_s_setprio(3);
  u32x4 ar[2][2], ai[2][2];
  f32x16 re, im;
#pragma unroll
  for (int tau = 0; tau < 2; ++tau) {
    u32x4 zr[2], zi[2];
#pragma unroll
    for (int s = 0; s < 2; ++s) { zr[s] = __builtin_bit_cast(u32x4, lds_b128(LX + tau * 4096 + s * 2048)); zi[s] = __builtin_bit_cast(u32x4, lds_b128(LX + tau * 4096 + s * 2048 + 16)); }
    if (special) { zi[0] = (u32x4){0, 0, 0, 0}; zi[1] = zi[0]; }
    SBAR();
    if (tau == 0) cmm<-1, true>(re, im, zr, zi, D); else cmm<-1, true>(re, im, zr, zi, T);
    pack2(ar[tau], re * twc + im * tws); pack2(ai[tau], im * twc - re * tws);
    SBAR();
  }
  asm volatile("s_waitcnt vmcnt(0)" ::: "memory");
  u32x4 ur[2][2], ui[2][2];
  {
    FT6(T, 28);
    u32x4 wv[8];
#pragma unroll
    for (int g = 0; g < 8; ++g) wv[g] = __builtin_bit_cast(u32x4, FR(52 + g));
    SBAR();
    f32x16 ore, oim;
    cmm<-1, false>(re, im, ar[0], ai[0], D);
    cmm<-1, false>(ore, oim, ar[1], ai[1], T);
    f32x16 wr_, wi_;
#define UNP(B) _Pragma("unroll") for (int g = 0; g < 4; ++g) _Pragma("unroll") for (int e = 0; e < 4; ++e) { wr_[4 * g + e] = __uint_as_float(wv[B + g][e] << 16); wi_[4 * g + e] = __uint_as_float(wv[B + g][e] & 0xffff0000u); }
    UNP(4);
    const f32x16 z1r = re - ore, z1i = im - oim;
    const f32x16 u1r = z1r * wr_ - z1i * wi_, u1i = z1r * wi_ + z1i * wr_;
    UNP(0);
    const f32x16 z0r = re + ore, z0i = im + oim;
    const f32x16 u0r = z0r * wr_ - z0i * wi_, u0i = z0r * wi_ + z0i * wr_;
    pack2(ur[0], u0r + u1r); pack2(ui[0], u0i + u1i); pack2(ur[1], u0r - u1r); pack2(ui[1], u0i - u1i);
    SBAR();
  }
  const float osc = special ? 0.5f : 1.f;
  FT6(T, 34);
#pragma unroll
  for (int kap = 0; kap < 2; ++kap) {
    bf16x8 T4[6];
    FT6(T4, 40 + kap * 6);
    SBAR();
    if (kap == 0) cmm<1, true>(re, im, ur[0], ui[0], D); else cmm<1, true>(re, im, ur[1], ui[1], T);
    u32x4 br[2], bi[2];
    pack2(br, re * twc - im * tws); pack2(bi, im * twc + re * tws);
    cmm<1, false>(re, im, br, bi, T4);
    re *= osc;
    { const unsigned ya = l0 + (lane & 31) * 128 + kap * 64 + (lane >> 5) * 32;
      *(LDS_AS u32x4*)(size_t)ya = pack8<0>(re); *(LDS_AS u32x4*)(size_t)(ya + 16) = pack8<1>(re);
      *(LDS_AS u32x4*)(size_t)(ya + 4096) = pack8<0>(im); *(LDS_AS u32x4*)(size_t)(ya + 4096 + 16) = pack8<1>(im); }
    SBAR();
  }
  { const char* yd = ws + WS_Y + (size_t)cr * 4096;
    u32x4 v[8];
#pragma unroll
    for (int i = 0; i < 8; ++i) v[i] = __builtin_bit_cast(u32x4, FR(i));
#pragma unroll
    for (int i = 0; i < 4; ++i) st16(vo + i * 1024, yd, v[i]);
    if (!special) {
#pragma unroll
      for (int i = 4; i < 8; ++i) st16(vo + i * 1024, yd, v[i]); } }
}

#define N0 13
#define N1 17
#define GLD(dst, voff, sb, imm) asm volatile("global_load_dwordx4 %0, %1, %2 offset:" #imm : "=v"(dst) : "v"(voff), "s"(sb) : "memory")
__global__ __launch_bounds__(512, 2) void fused_kernel(const char* __restrict__ x, const char* __restrict__ wqkv, const char* __restrict__ gw, const char* __restrict__ wr,
                                                        const char* __restrict__ wi, const char* __restrict__ wo, char* __restrict__ ws, char* __restrict__ out) {
  __shared__ __attribute__((aligned(1024))) char lds[163840];
  const int tid = threadIdx.x, lane = tid & 63, r = lane & 31, hh = lane >> 5;
  const int w8 = __builtin_amdgcn_readfirstlane(tid >> 6), w = w8 & 3;
  const int b = blockIdx.x, xcd = b & 7, bi = b >> 3;
  const unsigned l0 = (unsigned)(size_t)lds;
  const unsigned vo = lane * 16u;
  STAMP_DECL; STAMP(0);
  unsigned long long tstart = __builtin_amdgcn_s_memrealtime();
  const bool aux = bi >= 28;
  if (aux) {
    unsigned long long pc; unsigned rel;
    asm volatile("s_getpc_b64 %0\n\ts_mov_b32 %1, _Z12fused_kernelPKcS0_S0_S0_S0_S0_PcS1_@rel32@lo+4" : "=s"(pc), "=s"(rel));
    const unsigned ln = ((bi & 3) * 8 + w8) * 8 + (lane & 7);
    touch(l0 + 65536 + w8 * 256, (ln < CODE_LINES ? ln : 0) * 128u, (const char*)(pc + (long long)(int)rel));
  }
  const int h = 2 * xcd + (bi & 1), t2 = bi >> 1;
  u32x2 ep;
  {
    const int t = 4 * (xcd >> 1) + (bi & 3), g = 7 * (xcd & 1) + (bi >> 2);
    int rot = (bi >> 2) + 2 * (bi & 3) + ((xcd >> 1) & 1); rot = rot >= 7 ? rot - 7 : rot;
    const unsigned xo = (unsigned)(lane >> 3) * (64u * 896u) + (unsigned)(((lane & 7) ^ (lane >> 4)) * 16) + (unsigned)(4 * t + w) * 896u;
#define XLD(SLOT, C) do { const unsigned v_ = xo + (C) * 128u; const unsigned l_ = l0 + (SLOT) * K1_SLOT + w * 4096;                     \
    glds(l_, v_, x); glds(l_ + 1024, (v_ ^ 64u) + 458752u, x); glds(l_ + 2048, v_ + 917504u, x); glds(l_ + 3072, (v_ ^ 64u) + 1376256u, x); } while (0)
    if (!aux && w8 < 4) XLD(0, rot);
    const int c0 = 128 * g + 64 * (w >> 1);
    const unsigned rs = c0 < 1344 ? 2688u : 896u;
    const char* cb = c0 < 1344 ? wqkv : (const char*)&g_ftab - 2688;
    const unsigned co = (unsigned)((lane >> 3) + 32 * (w & 1)) * rs + (unsigned)(((lane & 7) ^ (4 * ((lane >> 4) & 1))) * 16) + c0 * 2u;
#define BLD(SLOT, C) do { const unsigned v_ = co + (C) * 64u * rs; const unsigned l_ = l0 + (SLOT) * K1_SLOT + 16384 + w * 4096;          \
    glds(l_, v_, cb); glds(l_ + 1024, v_ + 8u * rs, cb); glds(l_ + 2048, v_ + 16u * rs, cb); glds(l_ + 3072, v_ + 24u * rs, cb); } while (0)
    if (!aux && w8 < 4) BLD(0, rot);
    if (aux) {
      const char* seg = (w8 < 4 ? wr : wi) + (size_t)(64 * (xcd * 4 + (bi & 2)) + 32 * w + 16 * (bi & 1)) * 450;
      glds4(l0 + w8 * 8192, vo, seg); glds4(l0 + w8 * 8192 + 3104, vo, seg + 3104);
    }
#define TABS() _Pragma("unroll 1") for (int i = w; i < 11; i += 4) glds4(l0 + 102400 + 8192 + i * 4096, vo, (const char*)&g_fft + i * 4096)
#define EPLD() asm volatile("global_load_dwordx2 %0, %1, %2 sc1" : "=v"(ep) : "v"(0u), "s"(ws + WS_EP) : "memory")
#define MARK() do { const char* m_ = ws + WS_O + (size_t)((4 * t2 + w) * 16 + h) * 2048; st16(vo, m_, (u32x4){~0u, ~0u, ~0u, ~0u}); st16(vo + 1024, m_, (u32x4){~0u, ~0u, ~0u, ~0u}); } while (0)
#define DESTS(OFF, PO2) do { const unsigned tile0_ = 4 * t + 2 * (w & 1);                                                                 \
      if (g < 7) { const bool isq_ = c0 < 448; const unsigned hs_ = isq_ ? 131072u : 262144u; PO2 = isq_ ? 2048u : 4096u;                  \
        _Pragma("unroll") for (int j = 0; j < 4; ++j) { const int g8 = (c0 - (isq_ ? 0 : 448)) / 8 + 4 * (j >> 1) + 2 * hh + (j & 1), hp = (g8 * 37) >> 8, pos = g8 - 7 * hp; \
          OFF[j] = (isq_ ? WS_Q : WS_KV) + tile0_ * PO2 + r * 16u + (unsigned)(2 * hp + (pos > 3)) * hs_ + (unsigned)(pos > 3 ? pos - 3 : pos) * 512u; } } \
      else { const unsigned ts_ = c0 < 1344 ? 4096u : 2048u; PO2 = 1024u;                                                                 \
        _Pragma("unroll") for (int ct = 0; ct < 2; ++ct) { unsigned o_;                                                                   \
          if (c0 < 1344) { const int m = c0 + 32 * ct + r - 896, hd = (m * 2341) >> 16, d = m - 28 * hd; o_ = WS_KV + hd * 262144u + 2048u + (unsigned)(d + 32 * hh) * 16u; } \
          else o_ = WS_X + (unsigned)(((c0 - 1344) >> 5) + ct) * 131072u + vo;                                                            \
          OFF[2 * ct] = o_ + tile0_ * ts_; OFF[2 * ct + 1] = OFF[2 * ct] + ts_; } } } while (0)
    if (!aux && w8 < 4) {
#define issue(I) do { int c_ = (I) + rot; c_ = c_ >= 7 ? c_ - 7 : c_; XLD((I) % 5, c_); BLD((I) % 5, c_); } while (0)
      issue(1);
      asm volatile("s_waitcnt vmcnt(8)\n\ts_barrier" ::: "memory");
      STAMP(1);
      issue(2);
      asm volatile("s_waitcnt vmcnt(8)\n\ts_barrier" ::: "memory");
      EPLD();
      issue(3); issue(4);
      MARK();
      asm volatile("s_waitcnt vmcnt(16)\n\ts_barrier" ::: "memory");
      issue(5);
      asm volatile("s_waitcnt vmcnt(16)\n\ts_barrier" ::: "memory");
      issue(6);
      asm volatile("s_waitcnt vmcnt(16)\n\ts_barrier" ::: "memory");
      asm volatile("s_waitcnt vmcnt(8)\n\ts_barrier" ::: "memory");
      asm volatile("s_waitcnt vmcnt(0)\n\ts_barrier" ::: "memory");
      STAMP(2);
      TABS();
      if (g >= 7 && g <= 10) {
        const int m0 = 128 * (g - 7);
        for (int hd = m0 / 28 + w; hd < 16 && 28 * hd + 27 < m0 + 128; hd += 4) {
          const unsigned pv = (lane & 3) == 0 ? 0x3F803F80u : 0u;
          st16((unsigned)(lane >> 4) * 4096u + (unsigned)((lane >> 2) & 3) * 512u + (unsigned)(28 + (lane & 3)) * 16u, ws + WS_KV + (size_t)(hd * 64 + 4 * t) * 4096 + 2048, (u32x4){pv, pv, pv, pv});
        }
      }
    } else if (aux) {
      const int id = xcd * 4 + (bi - 28), fq = id >> 1, f1h = id & 1;
      touch(l0 + 65536 + w8 * 256, (((bi - 28) * 8 + w8) * 11 + (lane < 11 ? lane : 0)) * 128u, &g_fft);
      { const unsigned ln = (id * 8 + w8) * 13 + (lane < 13 ? lane : 0), lo = (ln < 3136 ? ln : 0) * 128u;
        touch(l0 + 65536 + w8 * 256, lo, wo); touch(l0 + 65536 + w8 * 256, lo, &g_gtab); }
      EPLD();
      asm volatile("s_waitcnt vmcnt(4)\n\ts_barrier" ::: "memory");
      STAMP(1);
      { const int f1l = tid & 15, k0 = tid >> 4;
        const LDS_AS unsigned short* src = (const LDS_AS unsigned short*)(size_t)(l0 + f1l * 450 + k0 * 2);
        const unsigned dofs = (unsigned)k0 * 8192u + (unsigned)(fq >> 1) * 1024u + (unsigned)(32 * (fq & 1) + 16 * f1h + f1l) * 16u;
        u32x4 v[8];
#pragma unroll
        for (int it = 0; it < 8; ++it)
#pragma unroll
          for (int e = 0; e < 4; ++e) v[it][e] = (unsigned)src[e * 4096 + it * 32] | ((unsigned)src[16384 + e * 4096 + it * 32] << 16);
#pragma unroll
        for (int it = 0; it < 8; ++it) if (it < 7 || k0 == 0) st16(dofs + it * 262144u, ws + WS_WT, v[it]); }
      const int gp = 64 * id + 8 * w8 + (lane >> 3);
      u32x4 gx[7], gv[7];
      { const unsigned gxo = (unsigned)(64 * (gp & 31) + (gp >> 5)) * 896u + (lane & 7) * 16u, go = (lane & 7) * 16u;
#define GLDG(i) asm volatile("global_load_dwordx4 %0, %2, %4 offset:" #i "*128\n\tglobal_load_dwordx4 %1, %3, %5 offset:" #i "*128" : "=&v"(gx[i]), "=&v"(gv[i]) : "v"(gxo), "v"(go), "s"(x), "s"(gw) : "memory")
        GLDG(0); GLDG(1); GLDG(2); GLDG(3); GLDG(4); GLDG(5); GLDG(6); }
      asm volatile("s_waitcnt vmcnt(14)\n\ts_barrier" : "+v"(ep)::"memory");
      if (tid == 0) __hip_atomic_store((gu64*)(ws + WS_PF + (224u + id) * 128), (((unsigned long long)ep[1] << 32) | ep[0]) + TAGINC, __ATOMIC_RELAXED, __HIP_MEMORY_SCOPE_AGENT);
      asm volatile("s_waitcnt vmcnt(0)" : "+v"(gx[0]), "+v"(gx[1]), "+v"(gx[2]), "+v"(gx[3]), "+v"(gx[4]), "+v"(gx[5]), "+v"(gx[6]),
                   "+v"(gv[0]), "+v"(gv[1]), "+v"(gv[2]), "+v"(gv[3]), "+v"(gv[4]), "+v"(gv[5]), "+v"(gv[6])::"memory");
      if (w8 < 4) { MARK(); TABS(); }
      STAMP(2);
      float z = 0.f;
#pragma unroll
      for (int i = 0; i < 7; ++i)
#pragma unroll
        for (int e = 0; e < 4; ++e) { const unsigned xa_ = gx[i][e], ga_ = gv[i][e]; z = __builtin_amdgcn_fdot2_f32_bf16(__builtin_bit_cast(bf16x2_t, xa_), __builtin_bit_cast(bf16x2_t, ga_), z, false); }
      z += __shfl_xor(z, 1); z += __shfl_xor(z, 2); z += __shfl_xor(z, 4);
      if ((lane & 7) == 0) __hip_atomic_store((gu32*)(ws + WS_G + (size_t)gp * 4), __float_as_uint(rbf(1.f / (1.f + __expf(-rbf(z))))), __ATOMIC_RELAXED, __HIP_MEMORY_SCOPE_AGENT);
    } else {
      __builtin_amdgcn_s_sleep(24);
      EPLD();
      const int wr_ = w & 1, wc = w >> 1;
      f32x16 acc[2][2] = {};
      const int q = (lane & 15) >> 2, p = lane & 3, G = (lane >> 4) & 1;
      const unsigned xa = l0 + wr_ * 8192 + r * 128 + ((hh ^ ((r >> 1) & 7)) * 16);
      const unsigned wb = l0 + 16384 + wc * 8192 + (8 * hh + q) * 128 + (q >> 1) * 64;
      const auto wsr = __builtin_amdgcn_make_buffer_rsrc((void*)ws, 0, (int)WS_WT, 0x00020000);
#define BST(V, VOFF) __builtin_amdgcn_raw_buffer_store_b128((V), wsr, (VOFF), 0, 16)
      const unsigned tile0 = 4 * t + 2 * wr_;
      if (g < 7) {
        const bool isq = c0 < 448;
        const unsigned padv = isq ? 0u : 0x3F80u;
        const unsigned hs = isq ? 131072u : 262144u, ts = isq ? 2048u : 4096u;
        unsigned off[4], of2[4];
#pragma unroll
        for (int j = 0; j < 4; ++j) {
          const int g8 = (c0 - (isq ? 0 : 448)) / 8 + 4 * (j >> 1) + 2 * hh + (j & 1);
          const int hp = (g8 * 37) >> 8, pos = g8 - 7 * hp;
          off[j] = (isq ? WS_Q : WS_KV) + tile0 * ts + r * 16u + (unsigned)(2 * hp + (pos > 3)) * hs + (unsigned)(pos > 3 ? pos - 3 : pos) * 512u;
          of2[j] = pos == 3 ? off[j] + hs - 1536u : 0x80000000u;
          PIN(off[j]); PIN(of2[j]);
        }
        k1_compute<1>(xa, wb + (4 * (p & 1) + 2 * G + (p >> 1)) * 8, acc, ws, off, ts);
        tstart = __builtin_amdgcn_s_memrealtime();
        STAMP(2);
        if (isq) {
#pragma unroll
          for (int j = 0; j < 4; ++j) acc[j >> 1][j & 1] *= 0.1889822365046136f * 1.4426950408889634f; }
#pragma unroll
        for (int j = 0; j < 4; ++j) {
          const bool bn = of2[j] != 0x80000000u;
          u32x4 d0 = (j & 1) ? pack8<1>(acc[0][j >> 1]) : pack8<0>(acc[0][j >> 1]), d1 = (j & 1) ? pack8<1>(acc[1][j >> 1]) : pack8<0>(acc[1][j >> 1]);
          if (__any(bn)) {
            BST(((u32x4){padv, 0u, d0[2], d0[3]}), of2[j]); BST(((u32x4){padv, 0u, d1[2], d1[3]}), of2[j] + ts);
            d0[2] = bn ? padv : d0[2]; d0[3] = bn ? 0u : d0[3]; d1[2] = bn ? padv : d1[2]; d1[3] = bn ? 0u : d1[3]; }
          BST(d0, off[j]); BST(d1, off[j] + ts);
        }
      } else {
        unsigned off[2]; const unsigned ts = c0 < 1344 ? 4096u : 2048u;
#pragma unroll
        for (int ct = 0; ct < 2; ++ct) {
          if (c0 < 1344) { const int m = c0 + 32 * ct + r - 896, hd = (m * 2341) >> 16, d = m - 28 * hd; off[ct] = WS_KV + hd * 262144u + 2048u + (unsigned)(d + 32 * hh) * 16u; }
          else off[ct] = WS_X + (unsigned)(((c0 - 1344) >> 5) + ct) * 131072u + vo;
          off[ct] += tile0 * ts; PIN(off[ct]);
        }
        const unsigned po[4] = {off[0], off[0] + ts, off[1], off[1] + ts};
        k1_compute<0>(xa, wb + (4 * G + p) * 8, acc, ws, po, 1024u);
        tstart = __builtin_amdgcn_s_memrealtime();
        STAMP(2);
#pragma unroll
        for (int ct = 0; ct < 2; ++ct)
#pragma unroll
          for (int rt = 0; rt < 2; ++rt) { BST(pack8<0>(acc[rt][ct]), off[ct] + rt * ts); BST(pack8<1>(acc[rt][ct]), off[ct] + rt * ts + 1024u); }
      }
    }
  }
  unsigned pi0, pi1;
  { const int cs = 28 * h - 4 * (h & 1), ck = (hh ? 896 + 28 * h : 448 + cs) + ((lane & 16) ? (hh ? 27 : 31) : 0);
    pi0 = (unsigned)((ck >> 7) * 16 + (lane & 15)); pi1 = (unsigned)(((cs + (hh ? 31 : 0)) >> 7) * 16 + t2); PIN(pi0); PIN(pi1); }
  unsigned tlo, thi;
  { asm volatile("s_waitcnt vmcnt(0)" : "+v"(ep)::"memory");
    __builtin_amdgcn_s_waitcnt(0x0F70);
    const u32x2 e = ep;
    const unsigned long long tg = (((unsigned long long)(unsigned)__builtin_amdgcn_readfirstlane(e[1]) << 32) | (unsigned)__builtin_amdgcn_readfirstlane(e[0])) + TAGINC;
    tlo = (unsigned)tg; thi = (unsigned)(tg >> 32); }
  STAMP(3);
  BAR();
  STAMP(4);
  if (w8 == 4) {
    const unsigned me = (unsigned)((7 * (xcd & 1) + (bi >> 2)) * 16 + 4 * (xcd >> 1) + (bi & 3));
    if (lane == 0 && !aux) __hip_atomic_store((gu64*)(ws + WS_PF + me * 128), ((unsigned long long)thi << 32) | tlo, __ATOMIC_RELAXED, __HIP_MEMORY_SCOPE_AGENT);
    while (__builtin_amdgcn_s_memrealtime() - tstart < P_AT) __builtin_amdgcn_s_sleep(1);
    wait_p1(ws + WS_PF, pi0, pi1, tlo, thi);
  }
  STAMP(5);
  BAR();
  if (w8 < 4) {
    const int rb = 4 * xcd + bi / 7, cb = bi % 7, rt = w >> 1, ct = w & 1;
    const unsigned sw = (unsigned)(((lane & 7) ^ (4 * ((lane >> 4) & 1))) * 16);
    const int u = 32 * xcd + bi;
    if (w == 0) {
      __builtin_amdgcn_s_sleep(FFT_SLEEP);
      __builtin_amdgcn_s_setprio(2);
      if (u < 225) {
        wait_p1(ws + WS_PF, (unsigned)(((1344 + (u == 224 ? 1 : 2 * u)) >> 7) * 16 + (lane & 15)), 224u + r, tlo, thi);
        STAMP(6);
        fft_wave(u, lane, ws, l0 + 102400); }
    } else {
      __builtin_amdgcn_s_sleep(H_SLEEP);
      if (!aux) { const unsigned so = (unsigned)(lane >> 3) * 896u + sw; const char* sb = wo + cb * 128;
#pragma unroll 1
        for (int j = w - 1; j < 56; j += 3) { glds(l0 + j * 1024, so, sb + (size_t)j * 8 * 896); __builtin_amdgcn_s_sleep(2); } }
    }
    STAMP(7);
#define YFLAG() __hip_atomic_store((gu32*)(ws + WS_YF + u * 4), tlo, __ATOMIC_RELAXED, __HIP_MEMORY_SCOPE_AGENT)
    if (aux) {
      asm volatile("s_waitcnt vmcnt(0)" ::: "memory");
      if (w == 0 && lane == 0) YFLAG();
      STAMP(15); STAMP_OUT(); ENDPGM(); }
    u32x4 f[28];
    { const char* db = (const char*)&g_gtab + (size_t)(2 * cb + ct) * 28672;
#define GLD4(I) do { const char* d0 = db + (I) * 1024; GLD(f[I], vo, d0, 0); GLD(f[I + 1], vo, d0, 1024); GLD(f[I + 2], vo, d0, 2048); GLD(f[I + 3], vo, d0, 3072); __builtin_amdgcn_s_sleep(1); } while (0)
      GLD4(0); GLD4(4); GLD4(8); GLD4(12); GLD4(16); GLD4(20); GLD4(24); }
    if (w == 0) {
      asm volatile("s_waitcnt vmcnt(28)" ::: "memory");
      if (lane == 0) YFLAG(); }
    __builtin_amdgcn_s_setprio(2);
#define F8(I) "+v"(f[I]), "+v"(f[I + 1]), "+v"(f[I + 2]), "+v"(f[I + 3]), "+v"(f[I + 4]), "+v"(f[I + 5]), "+v"(f[I + 6])
    STAMP(8);
    BAR();
    const unsigned yso = (unsigned)(lane >> 3) * 4096u + sw + (unsigned)w * (14u * 8u * 4096u); const char* ysb = ws + WS_Y + rb * 128;
    bool passed_b2 = false;
#pragma unroll 1
    for (int spin = 0; spin < (1 << 16); ++spin) {
      u32x4 v_; asm volatile("global_load_dwordx4 %0, %1, %2 sc1\n\ts_waitcnt vmcnt(0)" : "=&v"(v_) : "v"(vo), "s"(ws + WS_YF) : "memory");
      if (__all(((v_[0] ^ tlo) | (v_[1] ^ tlo) | (v_[2] ^ tlo) | (v_[3] ^ tlo)) == 0u)) break;
      if (!passed_b2 && spin >= 1) { BAR(); passed_b2 = true; }
      __builtin_amdgcn_s_sleep(10);
    }
    STAMP(9);
#pragma unroll
    for (int j = 0; j < 14; ++j) glds_dev(l0 + 57344 + (w * 14 + j) * 1024, yso + j * 32768u, ysb);
    float g;
    asm volatile("global_load_dword %0, %1, %2 sc1" : "=v"(g) : "v"(r * 4u), "s"(ws + WS_G + (size_t)(rb * 64 + rt * 32) * 4) : "memory");
    if (b == 0 && tid == 64) __hip_atomic_store((gu64*)(ws + WS_EP), ((unsigned long long)thi << 32) | tlo, __ATOMIC_RELAXED, __HIP_MEMORY_SCOPE_AGENT);
    __builtin_amdgcn_s_setprio(0);
    if (!passed_b2) BAR();
    const int q = (lane & 15) >> 2, p = lane & 3, G = (lane >> 4) & 1;
    unsigned w0 = l0 + ct * 28672 + (4 * hh + q) * 128 + (((q >> 1) ^ ct) * 64) + (4 * (p & 1) + 2 * G + (p >> 1)) * 8, w2 = w0 - hh * 512, w1 = w0 ^ 64, w3 = w2 ^ 64;
    PIN(w0); PIN(w1); PIN(w2); PIN(w3);
#define WOFF(J) (((J) >> 1) * 3584 + ((J) & 1) * 2048)
#define WOF(M, J) lds_tr2(((M) ? w1 : w0) + WOFF(J), (((J) & 1) ? ((M) ? w3 : w2) : ((M) ? w1 : w0)) + WOFF(J) + 1024)
    bf16x8 wn[2][8];
#pragma unroll
    for (int e = 0; e < 8; ++e) { wn[0][e] = WOF(0, e); wn[1][e] = WOF(1, e); }
#pragma unroll
    for (int e = 0; e < 8; ++e) { PIN(wn[0][e]); PIN(wn[1][e]); }
    asm volatile("s_waitcnt vmcnt(0)" : F8(0), F8(7), F8(14), F8(21), "+v"(g)::"memory");
    float ga = g * (2.0f / (2048.f * 448.f)), g1 = rbf(1.f - g); PIN(ga); PIN(g1);
    STAMP(10);
    BAR();
    STAMP(11);
    f32x16 acca = {}, accb = {}, accs = {};
    { unsigned ya = l0 + 57344 + (8 * hh + q) * 128 + (((q >> 1) ^ rt) * 64) + G * 32 + p * 8; PIN(ya);
      bf16x8 y[4];
#pragma unroll
      for (int i = 0; i < 4; ++i) y[i] = lds_tr2(ya + i * 2048, ya + i * 2048 + 512);
#pragma unroll
      for (int i = 0; i < 28; ++i) { SBAR(); acca = MFMA(asbf(f[i]), y[i & 3], acca); SBAR(); if (i + 4 < 28) y[i & 3] = lds_tr2(ya + (i + 4) * 2048, ya + (i + 4) * 2048 + 512); } }
    SBAR();
    STAMP(12);
    const char* ob_ = ws + WS_O + (size_t)(2 * rb + rt) * 32768 + ct * 16384;
#define OLD(J, IMM) asm volatile("global_load_dwordx4 %0, %1, %2 offset:" #IMM " sc1" : "+v"(fo[J]) : "v"(vo), "s"(og) : "memory")
#define OLD4(GQ) do { const char* og = ob_ + (GQ) * 4096; OLD(4 * (GQ), 0); OLD(4 * (GQ) + 1, 1024); OLD(4 * (GQ) + 2, 2048); OLD(4 * (GQ) + 3, 3072); } while (0)
    u32x4 fo[16];
#pragma unroll
    for (int e = 0; e < 16; ++e) asm volatile("" : "=v"(fo[e]));
    OLD4(0); OLD4(1); OLD4(2); OLD4(3);
    acca *= ga;
#define MX4(Y) mx = max(max(mx, Y[0]), Y[1]), mx = max(max(mx, Y[2]), Y[3])
#define F4(I) "+v"(fo[I]), "+v"(fo[I + 1]), "+v"(fo[I + 2]), "+v"(fo[I + 3])
#define OGRP(GQ, N, RELOAD)                                                                                                    \
    asm volatile("s_waitcnt vmcnt(" #N ")" : F4(4 * GQ)::"memory");                                                            \
    _Pragma("unroll 1") for (int spin = 0; spin < (1 << 16); ++spin) {                                                         \
      unsigned mx = 0; MX4(fo[4 * GQ]); MX4(fo[4 * GQ + 1]); MX4(fo[4 * GQ + 2]); MX4(fo[4 * GQ + 3]);                         \
      if (__builtin_expect(!__any(mx == ~0u), 1)) break;                                                                       \
      asm volatile("s_waitcnt vmcnt(0)" ::: "memory");                                                                         \
      RELOAD;                                                                                                                  \
      asm volatile("s_waitcnt vmcnt(" #N ")" : F4(4 * GQ)::"memory");                                                          \
    }                                                                                                                          \
    _Pragma("unroll") for (int e = 0; e < 4; ++e) { const int k_ = (GQ & 1) * 4 + e; SBAR();                                   \
      accs = MFMA(wn[1][k_], asbf(fo[4 * GQ + e]), accs); accb = MFMA(wn[0][k_], asbf(fo[4 * GQ + e]), accb); SBAR();         \
      if (GQ < 2) { wn[0][k_] = WOF(0, 4 * GQ + 8 + e); wn[1][k_] = WOF(1, 4 * GQ + 8 + e); } }
    OGRP(0, 12, OLD4(0); OLD4(1); OLD4(2); OLD4(3))
    OGRP(1, 8, OLD4(1); OLD4(2); OLD4(3))
    OGRP(2, 4, OLD4(2); OLD4(3))
    OGRP(3, 0, OLD4(3))
    STAMP(13);
    f32x4* ex = (f32x4*)(lds + 114688) + lane;
#pragma unroll
    for (int e = 0; e < 4; ++e) ex[(w ^ 1) * 256 + e * 64] = (f32x4){accs[4 * e], accs[4 * e + 1], accs[4 * e + 2], accs[4 * e + 3]};
    f32x16 res = accb * g1 + acca;
    __builtin_amdgcn_s_waitcnt(0xC07F);
    BAR();
#pragma unroll
    for (int e = 0; e < 4; ++e) { const f32x4 v = ex[w * 256 + e * 64];
#pragma unroll
      for (int k_ = 0; k_ < 4; ++k_) res[4 * e + k_] = fmaf(v[k_], g1, res[4 * e + k_]); }
    u32x4* od_ = (u32x4*)(out + (size_t)(64 * r + 2 * rb + rt) * 896 + (size_t)(cb * 64 + ct * 32 + 16 * hh) * 2);
    od_[0] = pack8<0>(res); od_[1] = pack8<1>(res);
    STAMP(15); STAMP_OUT();
    ENDPGM();
  }
  __builtin_amdgcn_s_setprio(1);
  const int n = w == 0 ? N0 : N1;
  unsigned ko = (((w == 0 ? 0 : N0 + (w - 1) * N1) + t2) & 63) * 4096u;
#define KNEXT() ko = (ko + 4096u) & 0x3FFFFu
  const auto kvr = __builtin_amdgcn_make_buffer_rsrc((void*)(ws + WS_KV + (size_t)h * 262144), 0, 262144, 0x00020000);
  const auto qr = __builtin_amdgcn_make_buffer_rsrc((void*)(ws + WS_Q + (size_t)(h * 64 + 4 * t2) * 2048), 0, 8192, 0x00020000);
#define KV(off, idx) __builtin_bit_cast(u32x4, __builtin_amdgcn_raw_buffer_load_b128(kvr, vo + (idx) * 1024, (off), 16))
#define QL(tile, idx) __builtin_bit_cast(u32x4, __builtin_amdgcn_raw_buffer_load_b128(qr, vo + (idx) * 1024, ((tile) & 3) * 2048, 16))
#define KV4(K0, K1, V0, V1) do { K0 = KV(ko, 0); K1 = KV(ko, 1); V0 = KV(ko, 2); V1 = KV(ko, 3); KNEXT(); } while (0)
  const u32x4 kr0 = KV(4 * t2 * 4096, 0), kr1 = KV(4 * t2 * 4096, 1);
  u32x4 qa0 = QL(w + 1, 0), qa1 = QL(w + 1, 1), qb0 = QL(w + 2, 0), qb1 = QL(w + 2, 1), qc0 = QL(w + 3, 0), qc1 = QL(w + 3, 1), qd0 = QL(w, 0), qd1 = QL(w, 1);
  SBAR();
  u32x4 kA0, kA1, vA0, vA1, kB0, kB1, vB0, vB1, kC0, kC1, vC0, vC1, kD0, kD1, vD0, vD1 = {0, 0, 0, 0};
  KV4(kA0, kA1, vA0, vA1); SBAR();
  KV4(kB0, kB1, vB0, vB1); SBAR();
  KV4(kC0, kC1, vC0, vC1); SBAR();
  f32x16 oa = {}, ob = {}, oc = {}, od = {}, sa, sb;
  const f32x16 zero = {};
  u32x4 p0, p1 = {0, 0, 0, 0};
  PIN(oa); PIN(ob); PIN(oc); PIN(od); PIN(p1); PIN(vD1); SBAR();
  {
    const bool selA = (h & 1) && hh == 0, selB = !(h & 1) && hh == 1;
    f32x16 s0 = MFMA(asbf(kr0), asbf(qa0), zero); s0 = MFMA(asbf(kr1), asbf(qa1), s0);
    f32x16 s1 = MFMA(asbf(kr0), asbf(qb0), zero); s1 = MFMA(asbf(kr1), asbf(qb1), s1);
    f32x16 s2 = MFMA(asbf(kr0), asbf(qc0), zero); s2 = MFMA(asbf(kr1), asbf(qc1), s2);
    f32x16 s3 = MFMA(asbf(kr0), asbf(qd0), zero); s3 = MFMA(asbf(kr1), asbf(qd1), s3);
#define REF(S, Q0, Q1) do { const unsigned nm = pk(-max16(S), 0.f); Q0[0] = selA ? nm : Q0[0]; Q1[2] = selB ? nm : Q1[2]; } while (0)
    REF(s0, qa0, qa1);
    sa = MFMA(asbf(kA0), asbf(qa0), zero); sa = MFMA(asbf(kA1), asbf(qa1), sa);
    REF(s1, qb0, qb1); REF(s2, qc0, qc1); REF(s3, qd0, qd1);
  }
  sb = zero;
  PIN(sa); STAMP(6);
#define SLOT(S, N, KN0, KN1, QN0, QN1, OP, VP, OC, VC, LD1, LD2)                                                              \
  do {                                                                                                                        \
    SBAR();                                                                                                                   \
    EX(S[0]); EX(S[1]); p1[2] = pk(N[12], N[13]); EX(S[2]); EX(S[3]); p1[3] = pk(N[14], N[15]); PIN(S); PIN(p1); SBAR();      \
    N = MFMA(asbf(KN0), asbf(QN0), zero); PIN(N); SBAR();                                                                             \
    EX(S[4]); EX(S[5]); p0[0] = pk(S[0], S[1]); EX(S[6]); EX(S[7]); p0[1] = pk(S[2], S[3]); PIN(S); PIN(p0); SBAR();          \
    N = MFMA(asbf(KN1), asbf(QN1), N); PIN(N); SBAR();                                                                                \
    LD1;                                                                                                                      \
    EX(S[8]); EX(S[9]); p0[2] = pk(S[4], S[5]); EX(S[10]); EX(S[11]); p0[3] = pk(S[6], S[7]); PIN(S); PIN(p0); SBAR();        \
    OP = MFMA(asbf(VP), asbf(p1), OP); PIN(OP); SBAR();                                                                                \
    EX(S[12]); EX(S[13]); p1[0] = pk(S[8], S[9]); EX(S[14]); EX(S[15]); p1[1] = pk(S[10], S[11]); PIN(S); PIN(p1); SBAR();    \
    OC = MFMA(asbf(VC), asbf(p0), OC); PIN(OC); SBAR();                                                                                \
    LD2;                                                                                                                      \
  } while (0)
  unsigned ca = l0 + 114688 + vo + ((w + 1) & 3) * 12288, cb_ = l0 + 114688 + vo + ((w + 2) & 3) * 12288 + 4096, cc = l0 + 114688 + vo + ((w + 3) & 3) * 12288 + 8192, cr_ = l0 + 114688 + vo + w * 12288;
  PIN(ca); PIN(cb_); PIN(cc); PIN(cr_);
#define TILE(X, P, N, REFILL)                                                                                                 \
    SLOT(sa, sb, k##X##0, k##X##1, qb0, qb1, od, v##P##1, oa, v##X##0, (void)0, (void)0);                                     \
    REFILL;                                                                                                                   \
    SLOT(sb, sa, k##X##0, k##X##1, qc0, qc1, oa, v##X##1, ob, v##X##0, (void)0, (void)0);                                     \
    SLOT(sa, sb, k##X##0, k##X##1, qd0, qd1, ob, v##X##1, oc, v##X##0, (void)0, __builtin_amdgcn_s_waitcnt(0x0F78));          \
    SLOT(sb, sa, k##N##0, k##N##1, qa0, qa1, oc, v##X##1, od, v##X##0, (void)0, (void)0);
#define KVS(P) KV4(k##P##0, k##P##1, v##P##0, v##P##1)
  __builtin_amdgcn_s_waitcnt(0x0F78);
  TILE(A, D, B, KVS(D))
  TILE(B, A, C, KVS(A))
  int i = n - 5;
  do {
    TILE(C, B, D, KVS(B))
    TILE(D, C, A, KVS(C))
    TILE(A, D, B, KVS(D))
    TILE(B, A, C, KVS(A))
    i -= 4;
  } while (i > 0);
  STAMP(8);
  BAR();
#define PUT2(O, A, I) do { *(LDS_AS f32x4*)(size_t)(A + (I) * 1024) = (f32x4){O[4 * (I)], O[4 * (I) + 1], O[4 * (I) + 2], O[4 * (I) + 3]}; \
                           *(LDS_AS f32x4*)(size_t)(A + (I) * 1024 + 1024) = (f32x4){O[4 * (I) + 4], O[4 * (I) + 5], O[4 * (I) + 6], O[4 * (I) + 7]}; } while (0)
  SLOT(sa, sb, kD0, kD1, qa0, qa1, od, vB1, oa, vC0, (void)0, (void)0);
  SLOT(sb, sa, kA0, kA1, qa0, qa1, oa, vC1, oa, vD0, (void)0, (void)0);
  SLOT(sa, sb, kC0, kC1, qb0, qb1, oa, vD1, oa, vA0, (void)0, (void)0);
  SLOT(sb, sa, kD0, kD1, qb0, qb1, oa, vA1, ob, vC0, (void)0, (void)0);
  SLOT(sa, sb, kA0, kA1, qb0, qb1, ob, vC1, ob, vD0, PUT2(oa, ca, 0), (void)0);
  SLOT(sb, sa, kC0, kC1, qc0, qc1, ob, vD1, ob, vA0, PUT2(oa, ca, 2), (void)0);
  SLOT(sa, sb, kD0, kD1, qc0, qc1, ob, vA1, oc, vC0, (void)0, (void)0);
  SLOT(sb, sa, kA0, kA1, qc0, qc1, oc, vC1, oc, vD0, PUT2(ob, cb_, 0), (void)0);
  SLOT(sa, sb, kC0, kC1, qd0, qd1, oc, vD1, oc, vA0, PUT2(ob, cb_, 2), (void)0);
  SLOT(sb, sa, kD0, kD1, qd0, qd1, oc, vA1, od, vC0, (void)0, (void)0);
  SLOT(sa, sb, kA0, kA1, qd0, qd1, od, vC1, od, vD0, PUT2(oc, cc, 0), PUT2(oc, cc, 2));
  SBAR();
  EX(sb[0]); EX(sb[1]); p1[2] = pk(sa[12], sa[13]); EX(sb[2]); EX(sb[3]); p1[3] = pk(sa[14], sa[15]); PIN(sb); PIN(p1); SBAR();
  od = MFMA(asbf(vD1), asbf(p1), od); PIN(od); SBAR();
  EX(sb[4]); EX(sb[5]); p0[0] = pk(sb[0], sb[1]); EX(sb[6]); EX(sb[7]); p0[1] = pk(sb[2], sb[3]); PIN(sb); PIN(p0); SBAR();
  __builtin_amdgcn_s_waitcnt(0xC07F);
  BAR();
  f32x4 pv[12];
#pragma unroll
  for (int j = 0; j < 12; ++j) pv[j] = *(const LDS_AS f32x4*)(size_t)(cr_ + j * 1024);
  SBAR();
  EX(sb[8]); EX(sb[9]); p0[2] = pk(sb[4], sb[5]); EX(sb[10]); EX(sb[11]); p0[3] = pk(sb[6], sb[7]); PIN(sb); PIN(p0); SBAR();
  od = MFMA(asbf(vA0), asbf(p0), od); PIN(od); SBAR();
  EX(sb[12]); EX(sb[13]); p1[0] = pk(sb[8], sb[9]); EX(sb[14]); EX(sb[15]); p1[1] = pk(sb[10], sb[11]); PIN(sb); PIN(p1); SBAR();
  p1[2] = pk(sb[12], sb[13]); p1[3] = pk(sb[14], sb[15]);
#pragma unroll
  for (int j = 0; j < 4; ++j) { pv[j] += pv[4 + j]; pv[j] += pv[8 + j]; PIN(pv[j]); }
  SBAR();
  od = MFMA(asbf(vA1), asbf(p1), od);
  PIN(od); STAMP(9);
  f32x16 o = od;
#pragma unroll
  for (int j = 0; j < 4; ++j) { o[4 * j] += pv[j][0]; o[4 * j + 1] += pv[j][1]; o[4 * j + 2] += pv[j][2]; o[4 * j + 3] += pv[j][3]; }
  float l;
  { auto rr = __builtin_amdgcn_permlane32_swap(__float_as_uint(o[12]), __float_as_uint(o[12]), false, false); l = __uint_as_float(rr[1]); }
  f32x2 sm = (f32x2){o[0], o[1]};
#pragma unroll
  for (int j = 2; j < 16; j += 2) sm += (f32x2){o[j], o[j + 1]};
  const bool ok = (l >= 1.0e-12f) && (l <= 1.0e12f) && (fabsf(sm[0] + sm[1]) <= 3.0e38f);
  if (__builtin_expect(!__all(ok), 0)) {
    const u32x4 q0 = QL(w, 0), q1 = QL(w, 1);
    float m = -INFINITY;
#pragma unroll 1
    for (int j = 0; j < 64; ++j) { f32x16 s = MFMA(asbf(KV(j * 4096, 0)), asbf(q0), zero); s = MFMA(asbf(KV(j * 4096, 1)), asbf(q1), s); m = fmaxf(m, max16(s)); }
    m = halfmax(m);
    o = zero;
#pragma unroll 1
    for (int j = 0; j < 64; ++j) {
      f32x16 s = MFMA(asbf(KV(j * 4096, 0)), asbf(q0), zero); s = MFMA(asbf(KV(j * 4096, 1)), asbf(q1), s);
#pragma unroll
      for (int e = 0; e < 16; ++e) s[e] = __builtin_amdgcn_exp2f(s[e] - m);
      o = MFMA(asbf(KV(j * 4096, 2)), asbf(pack8<0>(s)), o); o = MFMA(asbf(KV(j * 4096, 3)), asbf(pack8<1>(s)), o);
    }
    auto rr = __builtin_amdgcn_permlane32_swap(__float_as_uint(o[12]), __float_as_uint(o[12]), false, false); l = __uint_as_float(rr[1]);
  }
  o *= __builtin_amdgcn_rcpf(l);
  { u32x4 o0 = pack8<0>(o), o1 = pack8<1>(o);
    if (hh == 1) { o1[2] = 0; o1[3] = 0; }
#pragma unroll
    for (int j = 0; j < 4; ++j) { o0[j] = min(o0[j], 0xFFFFFFFEu); o1[j] = min(o1[j], 0xFFFFFFFEu); }
    const char* odst = ws + WS_O + (size_t)((4 * t2 + w) * 16 + h) * 2048;
    st16(vo, odst, o0); st16(vo + 1024, odst, o1); }
  STAMP(15);
  BAR();
  STAMP_OUT();
}

extern "C" void kernel_launch(void* const* d_in, const int* in_sizes, int n_in, void* d_out, int out_size, void* d_ws, size_t ws_size, hipStream_t stream) {
  fused_kernel<<<256, 512, 0, stream>>>((const char*)d_in[0], (const char*)d_in[4], (const char*)d_in[6], (const char*)d_in[1], (const char*)d_in[2], (const char*)d_in[5],
                                        (char*)d_ws, (char*)d_out);
}
```
